```python
import torch

def _softmax(x, axis=-1):
    return torch.softmax(x, dim=axis)
def _gelu(x):
    return torch.nn.functional.gelu(x, approximate="tanh")
def _silu(x):
    return torch.nn.functional.silu(x)
def _sigmoid(x):
    return torch.sigmoid(x)
def _layernorm(x, axis=-1, eps=1e-5):
    m = x.mean(dim=axis, keepdim=True)
    v = x.var(dim=axis, keepdim=True, unbiased=False)
    return (x - m) / torch.sqrt(v + eps)
def _scatter_add(tgt, idx, src, axis=0):
    return tgt.clone().index_add_(axis, idx, src)
def _segment_sum(src, seg, n):
    out = torch.zeros((n,) + src.shape[1:], dtype=src.dtype)
    return out.index_add_(0, seg, src)
def _pad(x, pw):
    flat = tuple(v for pair in reversed(pw) for v in pair)
    return torch.nn.functional.pad(x, flat)

kernel_name = 'concat_heads__mha_block_cached__sliding_block_cached__decode'

def setup_inputs():

    _w = (lambda a: (lambda t: t.to(torch.bfloat16) if t.is_floating_point() else t)(torch.as_tensor(a)))
    def _A_setup():
        B, Sq, D = 1, 1, 512
        H, Sk, Dh = 32, 32768, 128
        return {
            "x": _w((lambda *s: torch.randn(*s, dtype=torch.bfloat16) * 0.8)(B, Sq, D)),
            "k_cache": _w((lambda *s: torch.randn(*s, dtype=torch.bfloat16) * 1.6)(B, H, Sk, Dh)),
            "v_cache": _w((lambda *s: torch.randn(*s, dtype=torch.bfloat16) * 0.2)(B, H, Sk, Dh)),
            "wq": _w((lambda *s: torch.randn(*s, dtype=torch.bfloat16) * 1.6)(D, H * Dh) * (D ** -0.5)),
            "wo": _w((lambda *s: torch.randn(*s, dtype=torch.bfloat16) * 0.2)(H * Dh, D) * ((H * Dh) ** -0.5)),
        }
    _A_in = _A_setup()

    def _B_setup():
        B, Sq, D = 1, 1, 512
        Hq, Hkv, Sk, Dh = 32, 8, 32768, 128
        assert Hq % Hkv == 0, (Hq, Hkv)
        return {
            "x": _w((lambda *s: torch.randn(*s, dtype=torch.bfloat16) * 0.8)(B, Sq, D)),
            "k_cache": _w((lambda *s: torch.randn(*s, dtype=torch.bfloat16) * 1.6)(B, Hkv, Sk, Dh)),
            "v_cache": _w((lambda *s: torch.randn(*s, dtype=torch.bfloat16) * 0.2)(B, Hkv, Sk, Dh)),
            "wq": _w((lambda *s: torch.randn(*s, dtype=torch.bfloat16) * 1.6)(D, Hq * Dh) * (D ** -0.5)),
            "wo": _w((lambda *s: torch.randn(*s, dtype=torch.bfloat16) * 0.2)(Hq * Dh, D) * ((Hq * Dh) ** -0.5)),
        }
    _B_in = _B_setup()

    return {
        "A_x": _A_in["x"],
        "A_k_cache": _A_in["k_cache"],
        "A_v_cache": _A_in["v_cache"],
        "A_wq": _A_in["wq"],
        "A_wo": _A_in["wo"],
        "B_x": _B_in["x"],
        "B_k_cache": _B_in["k_cache"],
        "B_v_cache": _B_in["v_cache"],
        "B_wq": _B_in["wq"],
        "B_wo": _B_in["wo"],
    }

def reference(A_x, A_k_cache, A_v_cache, A_wq, A_wo, B_x, B_k_cache, B_v_cache, B_wq, B_wo):
    def _A_ref(x, k_cache, v_cache, wq, wo):
        B, Sq, D = x.shape
        H, Dh = 32, 128
        q = (lambda x, *d: x.permute(*d))((x @ wq).reshape(B, Sq, H, Dh), 0, 2, 1, 3)
        scores = torch.einsum("bhqd,bhkd->bhqk", q, k_cache) * (Dh ** -0.5)
        p = _softmax(scores, axis=-1)
        o = torch.einsum("bhqk,bhkd->bhqd", p, v_cache)
        return (lambda x, *d: x.permute(*d))(o, 0, 2, 1, 3).reshape(B, Sq, H * Dh) @ wo

    def _B_ref(x, k_cache, v_cache, wq, wo):
        B, Sq, D = x.shape
        Hq, Dh = 32, 128
        Hkv, Sk = k_cache.shape[1], k_cache.shape[2]
        W = min(4096, max(256, Sk // 4))
        rep = Hq // Hkv
        q = (lambda x, *d: x.permute(*d))((x @ wq).reshape(B, Sq, Hq, Dh), 0, 2, 1, 3)
        k = (lambda x, n, axis: torch.repeat_interleave(x, n, dim=axis))(k_cache[:, :, Sk - W:, :], rep, axis=1)
        v = (lambda x, n, axis: torch.repeat_interleave(x, n, dim=axis))(v_cache[:, :, Sk - W:, :], rep, axis=1)
        scores = torch.einsum("bhqd,bhkd->bhqk", q, k) * (Dh ** -0.5)
        p = _softmax(scores, axis=-1)
        o = torch.einsum("bhqk,bhkd->bhqd", p, v)
        return (lambda x, *d: x.permute(*d))(o, 0, 2, 1, 3).reshape(B, Sq, Hq * Dh) @ wo

    a = _A_ref(A_x, A_k_cache, A_v_cache, A_wq, A_wo)
    b = _B_ref(B_x, B_k_cache, B_v_cache, B_wq, B_wo)
    return torch.concatenate([a, b], axis=-1)
```

```cpp
#include <hip/hip_runtime.h>
#include <hip/hip_bf16.h>
#include <stdint.h>

typedef unsigned u32;
typedef unsigned long long u64;
typedef u32 u32x4 __attribute__((ext_vector_type(4)));
typedef u32 u32x2 __attribute__((ext_vector_type(2)));
typedef float f32x4 __attribute__((ext_vector_type(4)));
typedef __bf16 bf16x2 __attribute__((ext_vector_type(2)));

#define P_NS 8
#define P_U 2
#define P_NB 2
#define P_PREWAIT 0
#define P_DIAG 0

constexpr int DM = 512, NH = 32, DH = 128, SK = 32768, WIN = 4096;
constexpr int NWG = 256, NW = 8, NTHR = NW * 64;
constexpr int NS = P_NS;
constexpr int U = P_U;
constexpr int NB = P_NB;
constexpr int T = 4096 / (NS * 4 * U);
constexpr u32 TB = U * 1024;
constexpr float C2 = 0.08838834764831845f * 1.4426950408889634f;

constexpr u32 WS_EPOCH = 0, WS_QG = 4096, WS_H1A = 131072, WS_H1B = 524288, WS_Y = 2097152, WS_END = 4194304, WS_DIAG = WS_END;
constexpr u32 REC = 144;
constexpr u32 OOB = 0x80000000u;
constexpr u32 SPIN_CAP = 1u << 22;

#define SB() __builtin_amdgcn_sched_barrier(0)
#define MEMBAR() asm volatile("" ::: "memory")

__device__ __forceinline__ float dot2(u32 a, u32 b, float c) {
  return __builtin_amdgcn_fdot2_f32_bf16(__builtin_bit_cast(bf16x2, a), __builtin_bit_cast(bf16x2, b), c, false);
}
template <int CTRL> __device__ __forceinline__ float dpp(float x) {
  return __builtin_bit_cast(float, __builtin_amdgcn_mov_dpp(__builtin_bit_cast(int, x), CTRL, 0xf, 0xf, true));
}
__device__ __forceinline__ float blo(u32 w) { return __builtin_bit_cast(float, w << 16); }
__device__ __forceinline__ float bhi(u32 w) { return __builtin_bit_cast(float, w & 0xffff0000u); }
__device__ __forceinline__ float ex2(float x) { return __builtin_amdgcn_exp2f(x); }
__device__ __forceinline__ float x16_sum(float x) {
  auto s = __builtin_amdgcn_permlane16_swap(__float_as_uint(x), __float_as_uint(x), false, false);
  return __uint_as_float(s[0]) + __uint_as_float(s[1]);
}
__device__ __forceinline__ float x32_sum(float x) {
  auto s = __builtin_amdgcn_permlane32_swap(__float_as_uint(x), __float_as_uint(x), false, false);
  return __uint_as_float(s[0]) + __uint_as_float(s[1]);
}
__device__ __forceinline__ float x16_max(float x) {
  auto s = __builtin_amdgcn_permlane16_swap(__float_as_uint(x), __float_as_uint(x), false, false);
  return fmaxf(__uint_as_float(s[0]), __uint_as_float(s[1]));
}
__device__ __forceinline__ float x32_max(float x) {
  auto s = __builtin_amdgcn_permlane32_swap(__float_as_uint(x), __float_as_uint(x), false, false);
  return fmaxf(__uint_as_float(s[0]), __uint_as_float(s[1]));
}
__device__ __forceinline__ u32 f2bf(float f) {
  return (u32)__builtin_bit_cast(unsigned short, (__bf16)f);
}

template <int UU>
__device__ __forceinline__ void attn_tile(const u32x4 (&K)[UU], const u32x4 (&V)[UU], const u32x4 q, float& m, float& l, float (&acc)[8]) {
  float s[UU];
#pragma unroll
  for (int u = 0; u < UU; ++u) {
    float d = dot2(K[u].x, q.x, 0.f);
    d = dot2(K[u].y, q.y, d); d = dot2(K[u].z, q.z, d); d = dot2(K[u].w, q.w, d);
    s[u] = d;
  }
#pragma unroll
  for (int u = 0; u < UU; ++u) s[u] += dpp<0x128>(s[u]);
#pragma unroll
  for (int u = 0; u < UU; ++u) s[u] += dpp<0x124>(s[u]);
#pragma unroll
  for (int u = 0; u < UU; ++u) s[u] += dpp<0x122>(s[u]);
#pragma unroll
  for (int u = 0; u < UU; ++u) s[u] += dpp<0x121>(s[u]);
  float mx = s[0];
#pragma unroll
  for (int u = 1; u < UU; ++u) mx = fmaxf(mx, s[u]);
  const float mn = fmaxf(m, mx * C2);
  const float alpha = ex2(m - mn);
  m = mn;
  float p[UU], ps = 0.f;
#pragma unroll
  for (int u = 0; u < UU; ++u) { p[u] = ex2(fmaf(s[u], C2, -mn)); ps += p[u]; }
  l = fmaf(l, alpha, ps);
#pragma unroll
  for (int k = 0; k < 8; ++k) acc[k] *= alpha;
#pragma unroll
  for (int u = 0; u < UU; ++u) {
#pragma unroll
    for (int k = 0; k < 4; ++k) {
      acc[2 * k] = fmaf(p[u], blo(V[u][k]), acc[2 * k]);
      acc[2 * k + 1] = fmaf(p[u], bhi(V[u][k]), acc[2 * k + 1]);
    }
  }
}

__device__ __forceinline__ void park_state(float* st, int lane, float m, float l, float (&acc)[8]) {
  const float mw = x32_max(x16_max(m));
  const float sc = ex2(m - mw);
  const float lw = x32_sum(x16_sum(l * sc));
  float a[8];
#pragma unroll
  for (int k = 0; k < 8; ++k) a[k] = x32_sum(x16_sum(acc[k] * sc));
  if (lane < 16) {
    *reinterpret_cast<f32x4*>(st + 8 * lane) = f32x4{a[0], a[1], a[2], a[3]};
    *reinterpret_cast<f32x4*>(st + 8 * lane + 4) = f32x4{a[4], a[5], a[6], a[7]};
    if (lane == 0) { st[128] = mw; st[129] = lw; }
  }
}

struct Args {
  const unsigned short *xA, *kA, *vA, *wqA, *woA, *xB, *kB, *vB, *wqB, *woB;
  unsigned short* out;
  unsigned char* ws;
};

constexpr int ST = 132;
constexpr int L_ST = 0;
constexpr int L_Q = L_ST + 5 * NW * ST;
constexpr int L_RED = L_Q + 320;
constexpr int L_H1 = L_RED + NW * 32;
constexpr int L_END = L_H1 + 40 * 18;

__global__ void __launch_bounds__(NTHR, 2) fused_decode(Args a) {
  __shared__ __attribute__((aligned(16))) float lds[L_END];
  const int tid = threadIdx.x, lane = tid & 63, wave = __builtin_amdgcn_readfirstlane(tid >> 6);
  const int c = lane & 15;
  const int b = blockIdx.x, xg = b & 7, j = b >> 3;
  const int h = 4 * xg + (j >> 3), s = j & 7;
  const int g = xg;
  const auto rW = __builtin_amdgcn_make_buffer_rsrc((void*)a.ws, 0, WS_END, 0x00020000);
  const auto rK = __builtin_amdgcn_make_buffer_rsrc((void*)(a.kA + ((size_t)h * SK + (size_t)s * 4096) * DH), 0, 4096u * 256u, 0x00020000);
  const auto rV = __builtin_amdgcn_make_buffer_rsrc((void*)(a.vA + ((size_t)h * SK + (size_t)s * 4096) * DH), 0, 4096u * 256u, 0x00020000);

  const u32 yoff = WS_Y + (u32)((tid >> 8) * 256 + b) * 4096u + (u32)(tid & 255) * 16u;
  {
    const u32x4 z = {0u, 0u, 0u, 0u};
    __builtin_amdgcn_raw_buffer_store_b128(z, rW, yoff, 0, 16);
    const int rec = tid / 9, pc = tid - rec * 9;
    const u32 o = rec < 8 ? WS_H1A + (u32)((h * 8 + rec) * 8 + s) * REC : WS_H1B + (u32)((4 * g * 8 + (rec - 8)) * 32 + j) * REC;
    __builtin_amdgcn_raw_buffer_store_b128(z, rW, tid < 360 ? o + pc * 16u : OOB, 0, 16);
  }
  SB();
  const u32x2 ep = __builtin_amdgcn_raw_buffer_load_b64(rW, WS_EPOCH, 0, 16);
  const int col0 = 64 * (xg * 16 + (j >> 1)) + 32 * (j & 1);
  u32x4 wq[4]; u32x2 xv;
  {
    const unsigned short* W = (col0 < 4096 ? a.wqA : a.wqB) + (col0 & 4095);
    const unsigned short* X = col0 < 4096 ? a.xA : a.xB;
    const int rg = tid >> 2, cc = tid & 3;
    xv = *reinterpret_cast<const u32x2*>(X + 4 * rg);
#pragma unroll
    for (int i = 0; i < 4; ++i) wq[i] = __builtin_nontemporal_load(reinterpret_cast<const u32x4*>(W + (size_t)(4 * rg + i) * 4096 + 8 * cc));
  }
  SB();
  const int Tw = wave < NS ? T : 0;
  const u32 lane16 = lane * 16;
  u32x4 kb[NB][U], vb[NB][U];
  auto issue = [&](int bi, int t) {
    const u32 vo = t < Tw ? lane16 + (u32)(t * NS + wave) * TB : OOB;
#pragma unroll
    for (int u = 0; u < U; ++u) kb[bi][u] = __builtin_amdgcn_raw_buffer_load_b128(rK, vo + u * 1024, 0, 2);
#pragma unroll
    for (int u = 0; u < U; ++u) vb[bi][u] = __builtin_amdgcn_raw_buffer_load_b128(rV, vo + u * 1024, 0, 2);
  };
#pragma unroll
  for (int bi = 0; bi < NB - 1; ++bi) issue(bi, bi);
  SB();
  u32x4 kB[4], vB[4];
  {
    const size_t o = ((size_t)g * SK + (SK - WIN) + 128 * j + 16 * wave) * DH + lane * 8;
#pragma unroll
    for (int u = 0; u < 4; ++u) kB[u] = __builtin_nontemporal_load(reinterpret_cast<const u32x4*>(a.kB + o + u * 512));
#pragma unroll
    for (int u = 0; u < 4; ++u) vB[u] = __builtin_nontemporal_load(reinterpret_cast<const u32x4*>(a.vB + o + u * 512));
  }
  SB();
  {
    float qa[8];
#pragma unroll
    for (int k = 0; k < 8; ++k) qa[k] = 0.f;
#pragma unroll
    for (int i = 0; i < 4; ++i) {
      const float xi = (i & 1) ? bhi(xv[i >> 1]) : blo(xv[i >> 1]);
#pragma unroll
      for (int k = 0; k < 4; ++k) { qa[2 * k] = fmaf(xi, blo(wq[i][k]), qa[2 * k]); qa[2 * k + 1] = fmaf(xi, bhi(wq[i][k]), qa[2 * k + 1]); }
    }
#pragma unroll
    for (int k = 0; k < 8; ++k) { float v = qa[k]; v += dpp<0x124>(v); v += dpp<0x128>(v); qa[k] = x32_sum(x16_sum(v)); }
    if (lane < 4) {
      *reinterpret_cast<f32x4*>(lds + L_RED + wave * 32 + 8 * lane) = f32x4{qa[0], qa[1], qa[2], qa[3]};
      *reinterpret_cast<f32x4*>(lds + L_RED + wave * 32 + 8 * lane + 4) = f32x4{qa[4], qa[5], qa[6], qa[7]};
    }
  }
  const u64 tag64 = (((u64)ep.y << 32) | ep.x) + 1;
  const u32 tlo = (u32)tag64 << 16, thi = (u32)(tag64 >> 16);
  const u32 tag32 = (u32)tag64 | 0x80000000u;
  __syncthreads();
  if (tid < 32) {
    float v = 0.f;
#pragma unroll
    for (int w = 0; w < NW; ++w) v += lds[L_RED + w * 32 + tid];
    __builtin_amdgcn_raw_buffer_store_b64(u32x2{tlo | f2bf(v), thi}, rW, WS_QG + (u32)(col0 + tid) * 8u, 0, 16);
  }
  SB();
  u32 wo[16];
  {
    const unsigned short* W = (tid < 256 ? a.woA : a.woB) + (size_t)(128 * h + 16 * s) * DM + 2 * (tid & 255);
#pragma unroll
    for (int k = 0; k < 16; ++k) wo[k] = __builtin_nontemporal_load(reinterpret_cast<const u32*>(W + k * DM));
  }
  SB();
  {
    const u32 oB = WS_QG + (u32)(4096 + 512 * g + tid) * 8u;
    const u32 oA = tid < 128 ? WS_QG + (u32)(128 * h + tid) * 8u : OOB;
    u32x2 gB, gA;
    for (u32 spin = 0;; ++spin) {
      gB = __builtin_amdgcn_raw_buffer_load_b64(rW, oB, 0, 16);
      gA = __builtin_amdgcn_raw_buffer_load_b64(rW, oA, 0, 16);
      const bool ok = ((gB.x & 0xffff0000u) == tlo) && gB.y == thi && (tid >= 128 || (((gA.x & 0xffff0000u) == tlo) && gA.y == thi));
      if (__all(ok) || spin > SPIN_CAP) break;
      MEMBAR();
      __builtin_amdgcn_s_sleep(2);
    }
    unsigned short* lq = reinterpret_cast<unsigned short*>(lds + L_Q);
    lq[128 + tid] = (unsigned short)gB.x;
    if (tid < 128) lq[tid] = (unsigned short)gA.x;
  }
  __syncthreads();
  const u32x4 qA = *reinterpret_cast<const u32x4*>(lds + L_Q + 4 * c);

  float m = -INFINITY, l = 0.f, acc[8];
#pragma unroll
  for (int k = 0; k < 8; ++k) acc[k] = 0.f;
  int t0 = 0;
  do {
#pragma unroll
    for (int bi = 0; bi < NB; ++bi) {
      const int t = t0 + bi;
      if (P_PREWAIT) { __builtin_amdgcn_s_waitcnt(0x0F70 | (((NB - 2) * 2 * U) & 15) | ((((NB - 2) * 2 * U) >> 4) << 14)); }
      issue((bi + NB - 1) % NB, t + NB - 1);
      SB();
      if (t < Tw) attn_tile<U>(kb[bi], vb[bi], qA, m, l, acc);
      SB();
    }
    if (t0 == 0) {
#pragma unroll 1
      for (int e = 0; e < 4; ++e) {
        const u32x4 qB = *reinterpret_cast<const u32x4*>(lds + L_Q + 64 + 64 * e + 4 * c);
        float mb = -INFINITY, lb = 0.f, ab[8];
#pragma unroll
        for (int k = 0; k < 8; ++k) ab[k] = 0.f;
        attn_tile<4>(kB, vB, qB, mb, lb, ab);
        park_state(lds + L_ST + ((1 + e) * NW + wave) * ST, lane, mb, lb, ab);
      }
      SB();
    }
    if (t0 == 4 * NB && wave == 0) {
      for (u32 spin = 0;; ++spin) {
        bool ok = true;
#pragma unroll
        for (int i = 0; i < 4; ++i) {
          const u32x2 gq = __builtin_amdgcn_raw_buffer_load_b64(rW, WS_QG + (u32)(lane + 64 * i) * 256u, 0, 16);
          ok = ok && ((gq.x & 0xffff0000u) == tlo) && gq.y == thi;
        }
        if (__all(ok) || spin > SPIN_CAP) break;
        MEMBAR();
        __builtin_amdgcn_s_sleep(2);
      }
      if (b == 0 && lane == 0) __builtin_amdgcn_raw_buffer_store_b64(u32x2{(u32)tag64, (u32)(tag64 >> 32)}, rW, WS_EPOCH, 0, 16);
      SB();
    }
    t0 += NB;
  } while (t0 < Tw);
  if (wave < NS) park_state(lds + L_ST + wave * ST, lane, m, l, acc);
  __syncthreads();
  auto merge_publish = [&](const float* st, int nst, int d, u32 recbase  , u32 cstride) {
    float M = -INFINITY;
    for (int w = 0; w < nst; ++w) M = fmaxf(M, st[w * ST + 128]);
    float L = 0.f, av = 0.f;
    for (int w = 0; w < nst; ++w) { const float wt = ex2(st[w * ST + 128] - M); L = fmaf(wt, st[w * ST + 129], L); av = fmaf(wt, st[w * ST + d], av); }
    const u32 r = recbase + (u32)(d >> 4) * cstride;
    __builtin_amdgcn_raw_buffer_store_b64(u32x2{__float_as_uint(av), tag32}, rW, r + (u32)(d & 15) * 8u, 0, 16);
    __builtin_amdgcn_raw_buffer_store_b128(u32x4{__float_as_uint(M), tag32, __float_as_uint(L), tag32}, rW, (d & 15) == 0 ? r + 128u : OOB, 0, 16);
  };
  if (tid < 128) merge_publish(lds + L_ST, NS, tid, WS_H1A + (u32)((h * 8) * 8 + s) * REC, 8 * REC);
  merge_publish(lds + L_ST + (1 + (tid >> 7)) * NW * ST, NW, tid & 127, WS_H1B + (u32)(((4 * g + (tid >> 7)) * 8) * 32 + j) * REC, 32 * REC);
  {
    const int rec = tid / 9, pc = tid - rec * 9;
    const u32 o = tid < 360 ? (rec < 8 ? WS_H1A + (u32)((h * 8 + s) * 8 + rec) * REC : WS_H1B + (u32)((h * 8 + s) * 32 + (rec - 8)) * REC) + pc * 16u : OOB;
    u32x4 v;
    for (u32 spin = 0;; ++spin) {
      v = __builtin_amdgcn_raw_buffer_load_b128(rW, o, 0, 16);
      const bool ok = tid >= 360 || (v.y == tag32 && v.w == tag32);
      if (__all(ok) || spin > SPIN_CAP) break;
      MEMBAR();
      __builtin_amdgcn_s_sleep(1);
    }
    if (tid < 360) {
      if (pc == 8) { lds[L_H1 + rec] = __uint_as_float(v.x); lds[L_H1 + 40 + rec] = __uint_as_float(v.z); }
      else { lds[L_H1 + 80 + rec * 16 + 2 * pc] = __uint_as_float(v.x); lds[L_H1 + 80 + rec * 16 + 2 * pc + 1] = __uint_as_float(v.z); }
    }
  }
  __syncthreads();
  {
    const int r0 = wave < 4 ? 0 : 8, np = wave < 4 ? 8 : 32;
    float M = -INFINITY;
    for (int p = 0; p < np; ++p) M = fmaxf(M, lds[L_H1 + r0 + p]);
    float L = 0.f, o = 0.f;
    for (int p = lane >> 4; p < np; p += 4) {
      const float wt = ex2(lds[L_H1 + r0 + p] - M);
      L = fmaf(wt, lds[L_H1 + 40 + r0 + p], L);
      o = fmaf(wt, lds[L_H1 + 80 + (r0 + p) * 16 + c], o);
    }
    L = x32_sum(x16_sum(L)); o = x32_sum(x16_sum(o));
    o = blo(f2bf(o / L));
    float y0 = 0.f, y1 = 0.f;
#pragma unroll
    for (int k = 0; k < 16; ++k) {
      const float ok = __builtin_bit_cast(float, __builtin_amdgcn_readlane(__builtin_bit_cast(int, o), k));
      y0 = fmaf(ok, blo(wo[k]), y0); y1 = fmaf(ok, bhi(wo[k]), y1);
    }
    __builtin_amdgcn_raw_buffer_store_b128(u32x4{__float_as_uint(y0), tag32, __float_as_uint(y1), tag32}, rW, yoff, 0, 16);
  }
  if (b < 64) {
    const u32 o = WS_Y + (u32)((b >> 5) * 256 + (tid >> 3)) * 4096u + (u32)((b & 31) * 8 + (tid & 7)) * 16u;
    u32x4 v[4];
    for (u32 spin = 0;; ++spin) {
      bool ok = true;
#pragma unroll
      for (int i = 0; i < 4; ++i) v[i] = __builtin_amdgcn_raw_buffer_load_b128(rW, o + i * 64u * 4096u, 0, 16);
#pragma unroll
      for (int i = 0; i < 4; ++i) ok = ok && v[i].y == tag32 && v[i].w == tag32;
      if (__all(ok) || spin > SPIN_CAP) break;
      MEMBAR();
      __builtin_amdgcn_s_sleep(1);
    }
    float y0 = (__uint_as_float(v[0].x) + __uint_as_float(v[1].x)) + (__uint_as_float(v[2].x) + __uint_as_float(v[3].x));
    float y1 = (__uint_as_float(v[0].z) + __uint_as_float(v[1].z)) + (__uint_as_float(v[2].z) + __uint_as_float(v[3].z));
    y0 += dpp<0x128>(y0); y1 += dpp<0x128>(y1);
    y0 = x32_sum(x16_sum(y0)); y1 = x32_sum(x16_sum(y1));
    if (lane < 8) { lds[L_RED + wave * 16 + 2 * lane] = y0; lds[L_RED + wave * 16 + 2 * lane + 1] = y1; }
    __syncthreads();
    if (tid < 16) {
      float y = 0.f;
#pragma unroll
      for (int w = 0; w < NW; ++w) y += lds[L_RED + w * 16 + tid];
      a.out[b * 16 + tid] = (unsigned short)f2bf(y);
    }
  }
}

extern "C" void kernel_launch(void* const* d_in, const int* in_sizes, int n_in, void* d_out, int out_size, void* d_ws, size_t ws_size,
                              hipStream_t stream) {
  Args a;
  a.xA = (const unsigned short*)d_in[0]; a.kA = (const unsigned short*)d_in[1]; a.vA = (const unsigned short*)d_in[2];
  a.wqA = (const unsigned short*)d_in[3]; a.woA = (const unsigned short*)d_in[4];
  a.xB = (const unsigned short*)d_in[5]; a.kB = (const unsigned short*)d_in[6]; a.vB = (const unsigned short*)d_in[7];
  a.wqB = (const unsigned short*)d_in[8]; a.woB = (const unsigned short*)d_in[9];
  a.out = (unsigned short*)d_out; a.ws = (unsigned char*)d_ws;
  hipLaunchKernelGGL(fused_decode, dim3(NWG), dim3(NTHR), 0, stream, a);
}
```

```cpp
#include <hip/hip_runtime.h>
#include <hip/hip_bf16.h>
#include <stdint.h>

typedef unsigned u32;
typedef unsigned long long u64;
typedef u32 u32x4 __attribute__((ext_vector_type(4)));
typedef u32 u32x2 __attribute__((ext_vector_type(2)));
typedef float f32x4 __attribute__((ext_vector_type(4)));
typedef float f32x2 __attribute__((ext_vector_type(2)));
typedef __bf16 bf16x2 __attribute__((ext_vector_type(2)));
typedef __bf16 bf16x8 __attribute__((ext_vector_type(8)));

#define P_U 2
#define P_NB 2
#define P_SPIN 20
#define P_DBG 0
#define P_DQ 24
#define P_HT 4
#define P_WSEND 0
#define P_RSLEEP 4

constexpr int DM = 512, DH = 128, SK = 32768, WIN = 4096;
constexpr int NWG = 256, NTHR = 512;
constexpr int U = P_U;
constexpr int NB = P_NB;
constexpr int DQ = P_DQ, HT = P_HT;
constexpr int HK = 4 * 16 * HT;
constexpr int UNIT = 16 * U;
constexpr u32 TB = U * 1024;
constexpr float SCALE = 0.08838834764831845f;
constexpr float LOG2E = 1.4426950408889634f;
constexpr int NUN = SK / UNIT, UQ = NUN / 7, UR = NUN % 7;

constexpr u32 WS_EPOCH = 0, WS_ML = 4096, WS_Y = 1048576, WS_END = 2097152, WS_DIAG = WS_END;
constexpr u32 OOB = 0x80000000u;
constexpr u32 SPIN_CAP = 1u << P_SPIN;

constexpr int L_QP = 0, L_AF = L_QP + 2048, AF_ROW = 272, L_ML = L_AF + 16 * AF_ROW, L_RED = L_ML + 64, L_CNT = L_RED + 512, L_END = L_CNT + 32;

#define SB() __builtin_amdgcn_sched_barrier(0)
#define MEMBAR() asm volatile("" ::: "memory")
#define WAIT_LDS() asm volatile("s_waitcnt lgkmcnt(0)" ::: "memory")
#define WAIT_VM() asm volatile("s_waitcnt vmcnt(0)" ::: "memory")

__device__ __forceinline__ float dot2(u32 a, u32 b, float c) {
  return __builtin_amdgcn_fdot2_f32_bf16(__builtin_bit_cast(bf16x2, a), __builtin_bit_cast(bf16x2, b), c, false);
}
template <int CTRL> __device__ __forceinline__ float dpp(float x) {
  return __builtin_bit_cast(float, __builtin_amdgcn_mov_dpp(__builtin_bit_cast(int, x), CTRL, 0xf, 0xf, true));
}
constexpr int ROR8 = 0x128, ROR4 = 0x124, ROR2 = 0x122, ROR1 = 0x121, QX2 = 0x4E  ;
__device__ __forceinline__ float blo(u32 w) { return __builtin_bit_cast(float, w << 16); }
__device__ __forceinline__ float bhi(u32 w) { return __builtin_bit_cast(float, w & 0xffff0000u); }
__device__ __forceinline__ float ex2(float x) { return __builtin_amdgcn_exp2f(x); }
__device__ __forceinline__ u32 fu(float x) { return __builtin_bit_cast(u32, x); }
__device__ __forceinline__ float uf(u32 x) { return __builtin_bit_cast(float, x); }
__device__ __forceinline__ u32 pk_bf16(float a, float b) {
  return __builtin_bit_cast(u32, __builtin_convertvector(f32x2{a, b}, bf16x2));
}
__device__ __forceinline__ float rs16(float a, float b) {
  auto s = __builtin_amdgcn_permlane16_swap(fu(a), fu(b), false, false);
  return uf(s[0]) + uf(s[1]);
}
__device__ __forceinline__ float rs32(float a, float b) {
  auto s = __builtin_amdgcn_permlane32_swap(fu(a), fu(b), false, false);
  return uf(s[0]) + uf(s[1]);
}
__device__ __forceinline__ float x16_max(float x) {
  auto s = __builtin_amdgcn_permlane16_swap(fu(x), fu(x), false, false);
  return fmaxf(uf(s[0]), uf(s[1]));
}
__device__ __forceinline__ float x32_max(float x) {
  auto s = __builtin_amdgcn_permlane32_swap(fu(x), fu(x), false, false);
  return fmaxf(uf(s[0]), uf(s[1]));
}
__device__ __forceinline__ void rscat8(const float (&v)[8], float& z0, float& z1) {
  const float w0 = rs32(v[0], v[4]), w1 = rs32(v[1], v[5]), w2 = rs32(v[2], v[6]), w3 = rs32(v[3], v[7]);
  z0 = rs16(w0, w2); z1 = rs16(w1, w3);
}

template <int UU>
__device__ __forceinline__ void attn_tile(const u32x4 (&K)[UU], const u32x4 (&V)[UU], const u32x4 q, float& m, float& mL, float& l, float (&acc)[8]) {
  float s[UU];
#pragma unroll
  for (int u = 0; u < UU; ++u) {
    float d = dot2(K[u].x, q.x, 0.f);
    d = dot2(K[u].y, q.y, d); d = dot2(K[u].z, q.z, d); d = dot2(K[u].w, q.w, d);
    s[u] = d;
  }
#pragma unroll
  for (int u = 0; u < UU; ++u) s[u] += dpp<ROR8>(s[u]);
#pragma unroll
  for (int u = 0; u < UU; ++u) s[u] += dpp<ROR4>(s[u]);
#pragma unroll
  for (int u = 0; u < UU; ++u) s[u] += dpp<ROR2>(s[u]);
#pragma unroll
  for (int u = 0; u < UU; ++u) s[u] += dpp<ROR1>(s[u]);
  float mx = m;
#pragma unroll
  for (int u = 0; u < UU; u += 2) {
    const u32 r1 = pk_bf16(s[u], s[u + 1]);
    const u32 r2 = pk_bf16(blo(r1) * SCALE, bhi(r1) * SCALE);
    s[u] = blo(r2); s[u + 1] = bhi(r2);
    mx = fmaxf(mx, fmaxf(s[u], s[u + 1]));
  }
  if (__any(mx > m)) {
    const float mLn = mx * LOG2E;
    const float alpha = ex2(mL - mLn);
    m = mx; mL = mLn;
    l *= alpha;
#pragma unroll
    for (int k = 0; k < 8; ++k) acc[k] *= alpha;
  }
#pragma unroll
  for (int u = 0; u < UU; u += 2) {
    const u32 pp = pk_bf16(ex2(fmaf(s[u], LOG2E, -mL)), ex2(fmaf(s[u + 1], LOG2E, -mL)));
    const u32 lo0 = pp & 0xffffu, hi0 = pp << 16, lo1 = pp >> 16, hi1 = pp & 0xffff0000u;
    l += uf(hi0) + uf(hi1);
#pragma unroll
    for (int k = 0; k < 4; ++k) {
      acc[2 * k] = dot2(V[u][k], lo0, acc[2 * k]);
      acc[2 * k + 1] = dot2(V[u][k], hi0, acc[2 * k + 1]);
    }
#pragma unroll
    for (int k = 0; k < 4; ++k) {
      acc[2 * k] = dot2(V[u + 1][k], lo1, acc[2 * k]);
      acc[2 * k + 1] = dot2(V[u + 1][k], hi1, acc[2 * k + 1]);
    }
  }
}

__device__ __forceinline__ void wave_merge(float& mL, float& l, float (&acc)[8], float& z0, float& z1) {
  const float mw = x32_max(x16_max(mL));
  const float sc = ex2(mL - mw);
  const float ls = l * sc;
  const float l1 = rs16(ls, ls);
  l = rs32(l1, l1);
  mL = mw;
#pragma unroll
  for (int k = 0; k < 8; ++k) acc[k] *= sc;
  rscat8(acc, z0, z1);
}

__device__ __forceinline__ void lds_arrive(u32* cnt, int lane) {
  MEMBAR();
  if (lane == 0) __hip_atomic_fetch_add(cnt, 1u, __ATOMIC_RELAXED, __HIP_MEMORY_SCOPE_WORKGROUP);
  MEMBAR();
}
__device__ __forceinline__ void lds_wait(u32* cnt, u32 n) {
  for (u32 spin = 0; spin < (1u << 24); ++spin) {
    if (__hip_atomic_load(cnt, __ATOMIC_RELAXED, __HIP_MEMORY_SCOPE_WORKGROUP) >= n) break;
    __builtin_amdgcn_s_sleep(1);
  }
  MEMBAR();
}

struct Args {
  const void* p[2][8];
};

#define STAMP(i)

__device__ __forceinline__ void park(unsigned char* lds, int wave, int lane, float mL, float l, float (&acc)[8]) {
  float z0, z1;
  wave_merge(mL, l, acc, z0, z1);
  const u32 hi = pk_bf16(z0, z1);
  const u32 lo = pk_bf16(z0 - blo(hi), z1 - bhi(hi));
  unsigned char* p = lds + L_AF + (2 * wave) * AF_ROW + 16 * (lane & 15) + 4 * (lane >> 4);
  *reinterpret_cast<u32*>(p) = hi;
  *reinterpret_cast<u32*>(p + AF_ROW) = lo;
  if (lane == 0) { reinterpret_cast<float*>(lds + L_ML)[wave] = mL; reinterpret_cast<float*>(lds + L_ML)[8 + wave] = l; }
}
__device__ __forceinline__ u32x4 read_q(const unsigned char* lds, int c) {
  f32x4 lo = *reinterpret_cast<const f32x4*>(lds + L_QP + 32 * c), hi = *reinterpret_cast<const f32x4*>(lds + L_QP + 32 * c + 16);
#pragma unroll
  for (int w = 1; w < 4; ++w) {
    lo += *reinterpret_cast<const f32x4*>(lds + L_QP + w * 512 + 32 * c);
    hi += *reinterpret_cast<const f32x4*>(lds + L_QP + w * 512 + 32 * c + 16);
  }
  return u32x4{pk_bf16(lo.x, lo.y), pk_bf16(lo.z, lo.w), pk_bf16(hi.x, hi.y), pk_bf16(hi.z, hi.w)};
}

__global__ void __launch_bounds__(NTHR, 2) fused_decode(Args a) {
  __shared__ __attribute__((aligned(16))) unsigned char lds[L_END];
  const int tid = threadIdx.x, lane = tid & 63, wave = __builtin_amdgcn_readfirstlane(tid >> 6);
  const int c = lane & 15, r = lane >> 4;
  const int b = blockIdx.x, xg = b & 7, j = b >> 3;
  const int h = 4 * xg + (j >> 3), s = j & 7;
  const int pidx = h * 8 + s;
  const bool isB = s == 7;
  u32* cnt = reinterpret_cast<u32*>(lds + L_CNT);
  const void* const* in = a.p[isB ? 1 : 0];
  const unsigned short* X = (const unsigned short*)in[0];
  const unsigned short* KC = (const unsigned short*)in[1];
  const unsigned short* VC = (const unsigned short*)in[2];
  const unsigned short* WQ = (const unsigned short*)in[3] + 128 * h;
  const unsigned short* WO = (const unsigned short*)in[4] + (size_t)128 * h * DM;
  unsigned short* const out = (unsigned short*)in[5];
  unsigned char* const ws = (unsigned char*)in[6];
  asm volatile("" :: "s"(X), "s"(KC), "s"(VC), "s"(WQ), "s"(WO), "s"(ws), "s"(out));
  const int un0 = s * UQ + (s < UR ? s : UR);
  const int nun = isB ? WIN / UNIT : UQ + (s < UR ? 1 : 0);
  const size_t row0 = isB ? (size_t)(h >> 2) * SK + (SK - WIN) : (size_t)h * SK + (size_t)un0 * UNIT;
  const auto rK = __builtin_amdgcn_make_buffer_rsrc((void*)(KC + row0 * DH), 0, (u32)nun * (UNIT * 256u), 0x00020000);
  const auto rV = __builtin_amdgcn_make_buffer_rsrc((void*)(VC + row0 * DH), 0, (u32)nun * (UNIT * 256u), 0x00020000);
  const auto rW = __builtin_amdgcn_make_buffer_rsrc((void*)ws, 0, WS_END, 0x00020000);
  const u32 lane16 = lane * 16;

  if (wave < 4) {
    const auto rQ = __builtin_amdgcn_make_buffer_rsrc((void*)WQ, 0, (u32)(DM * 4096 - 128 * h) * 2u, 0x00020000);
    const int T = nun - HK / UNIT;
    u32x4 xq[4], wq[32];
    u32 qoff[4];
    {
      const int rg = tid >> 4;
#pragma unroll
      for (int q = 0; q < 4; ++q) {
        const int rw = 32 * rg + 8 * ((q + s) & 3);
        xq[q] = __builtin_nontemporal_load(reinterpret_cast<const u32x4*>(X + rw));
        qoff[q] = (u32)rw * 8192u + 16u * c;
      }
    }
    auto ldq = [&](int i) { wq[i] = __builtin_amdgcn_raw_buffer_load_b128(rQ, qoff[i >> 3], (i & 7) * 8192, 2); };
    u32x4 kb[NB][U], vb[NB][U];
    auto issue = [&](int bi, int t) {
      const u32 vo = t < T ? lane16 + HK * 256u + (u32)(t * 4 + wave) * TB : OOB;
#pragma unroll
      for (int u = 0; u < U; ++u) kb[bi][u] = __builtin_amdgcn_raw_buffer_load_b128(rK, vo + u * 1024, 0, 2);
#pragma unroll
      for (int u = 0; u < U; ++u) vb[bi][u] = __builtin_amdgcn_raw_buffer_load_b128(rV, vo + u * 1024, 0, 2);
    };
#pragma unroll
    for (int i = 0; i < DQ; ++i) ldq(i);
    SB();
    __builtin_amdgcn_s_barrier();
    SB();
    {
      float qa[8];
#pragma unroll
      for (int k = 0; k < 8; ++k) qa[k] = 0.f;
#pragma unroll
      for (int i0 = 0; i0 < 32; i0 += 4) {
#pragma unroll
        for (int i = i0; i < i0 + 4; ++i) {
          const u32 xw = xq[i >> 3][(i & 7) >> 1];
          const u32 lo = (i & 1) ? xw >> 16 : xw & 0xffffu, hi = (i & 1) ? xw & 0xffff0000u : xw << 16;
#pragma unroll
          for (int k = 0; k < 4; ++k) { qa[2 * k] = dot2(wq[i][k], lo, qa[2 * k]); qa[2 * k + 1] = dot2(wq[i][k], hi, qa[2 * k + 1]); }
        }
        SB();
        if (i0 + DQ < 32) {
#pragma unroll
          for (int i = i0 + DQ; i < i0 + DQ + 4; ++i) ldq(i);
        }
        if (i0 + DQ == 32 || (DQ == 32 && i0 == 0)) {
#pragma unroll
          for (int bi = 0; bi < NB - 1; ++bi) issue(bi, bi);
        }
        SB();
      }
      float z0, z1;
      rscat8(qa, z0, z1);
      *reinterpret_cast<f32x2*>(lds + L_QP + wave * 512 + 32 * c + 8 * r) = f32x2{z0, z1};
    }
    lds_arrive(cnt, lane);
    lds_wait(cnt, 4);
    const u32x4 q = read_q(lds, c);
    float m = -INFINITY, mL = -INFINITY, l = 0.f, acc[8];
#pragma unroll
    for (int k = 0; k < 8; ++k) acc[k] = 0.f;
    int t0 = 0;
    do {
#pragma unroll
      for (int bi = 0; bi < NB; ++bi) {
        const int t = t0 + bi;
        issue((bi + NB - 1) % NB, t + NB - 1);
        SB();
        if (bi == 0 || t < T) attn_tile<U>(kb[bi], vb[bi], q, m, mL, l, acc);
        SB();
      }
      t0 += NB;
    } while (t0 < T);
    park(lds, wave, lane, mL, l, acc);
    WAIT_LDS();
    __builtin_amdgcn_s_barrier();
  } else {
    const int hw = wave - 4, th = tid - 256;
    const u32x2 ep = __builtin_amdgcn_raw_buffer_load_b64(rW, WS_EPOCH, 0, 16);
    if (th < 8) cnt[th] = 0;
    WAIT_LDS();
    SB();
    __builtin_amdgcn_s_barrier();
    SB();
    {
      u32x4 kk[HT][4], vv[HT][4];
#pragma unroll
      for (int t = 0; t < HT; ++t) {
        const u32 so = (u32)(t * 4 + hw) * 4096u;
#pragma unroll
        for (int u = 0; u < 4; ++u) kk[t][u] = __builtin_amdgcn_raw_buffer_load_b128(rK, lane16 + u * 1024, so, 2);
#pragma unroll
        for (int u = 0; u < 4; ++u) vv[t][u] = __builtin_amdgcn_raw_buffer_load_b128(rV, lane16 + u * 1024, so, 2);
      }
      SB();
      lds_wait(cnt, 4);
      const u32x4 q = read_q(lds, c);
      float m = -INFINITY, mL = -INFINITY, l = 0.f, acc[8];
#pragma unroll
      for (int k = 0; k < 8; ++k) acc[k] = 0.f;
#pragma unroll
      for (int t = 0; t < HT; ++t) { attn_tile<4>(kk[t], vv[t], q, m, mL, l, acc); SB(); }
      park(lds, wave, lane, mL, l, acc);
    }
    SB();
    u32x4 wo[4][8];
    {
      const auto rO = __builtin_amdgcn_make_buffer_rsrc((void*)WO, 0, 128u * 1024u, 0x00020000);
      const u32 vo = (P_DBG & 2) ? OOB : (u32)r * 8192u + (u32)hw * 256u + 16u * c;
#pragma unroll
      for (int st = 0; st < 4; ++st)
#pragma unroll
        for (int i = 0; i < 8; ++i) wo[st][i] = __builtin_amdgcn_raw_buffer_load_b128(rO, vo + (i & 3) * 1024, (st * 32 + (i & 4)) * 1024, 2);
    }
    const u64 tag64 = (((u64)ep.y << 32) | ep.x) + 1;
    const u32 tag32 = (u32)tag64 | 0x80000000u;
    if (th == 0) { cnt[2] = (u32)tag64; cnt[3] = (u32)(tag64 >> 32); }
    SB();
    u32x4 wf[4][8];
#pragma unroll
    for (int st = 0; st < 4; ++st)
#pragma unroll
      for (int t = 0; t < 8; ++t)
#pragma unroll
        for (int k = 0; k < 4; ++k)
          wf[st][t][k] = __builtin_amdgcn_perm(wo[st][2 * k + 1][t >> 1], wo[st][2 * k][t >> 1], (t & 1) ? 0x07060302u : 0x05040100u);
    SB();
    WAIT_LDS();
    __builtin_amdgcn_s_barrier();
    {
      const f32x4 ma = *reinterpret_cast<const f32x4*>(lds + L_ML), mb = *reinterpret_cast<const f32x4*>(lds + L_ML + 16);
      const f32x2 mr = *reinterpret_cast<const f32x2*>(lds + L_ML + 8 * r), lr = *reinterpret_cast<const f32x2*>(lds + L_ML + 32 + 8 * r);
      u32x4 af[4];
#pragma unroll
      for (int st = 0; st < 4; ++st) af[st] = *reinterpret_cast<const u32x4*>(lds + L_AF + c * AF_ROW + st * 64 + 16 * r);
      const float M = fmaxf(fmaxf(fmaxf(ma.x, ma.y), fmaxf(ma.z, ma.w)), fmaxf(fmaxf(mb.x, mb.y), fmaxf(mb.z, mb.w)));
      const float w0 = ex2(mr.x - M), w1 = ex2(mr.y - M);
      float L = w0 * lr.x + w1 * lr.y;
      L = rs16(L, L); L = rs32(L, L);
      __builtin_amdgcn_raw_buffer_store_b128(u32x4{fu(M), tag32, fu(L), tag32}, rW, th == 0 ? WS_ML + (u32)pidx * 16u : OOB, 0, 16);
      float y[8];
#pragma unroll
      for (int t = 0; t < 8; ++t) {
        f32x4 d = {0.f, 0.f, 0.f, 0.f};
#pragma unroll
        for (int st = 0; st < 4; ++st)
          d = __builtin_amdgcn_mfma_f32_16x16x32_bf16(__builtin_bit_cast(bf16x8, af[st]), __builtin_bit_cast(bf16x8, wf[st][t]), d, 0, 0, 0);
        y[t] = w0 * (d.x + d.y) + w1 * (d.z + d.w);
      }
      float z0, z1;
      rscat8(y, z0, z1);
      __builtin_amdgcn_raw_buffer_store_b128(u32x4{fu(z0), tag32, fu(z1), tag32}, rW, WS_Y + (u32)pidx * 4096u + (u32)th * 16u, 0, 16);
    }
  }
  if (b < 64) {
    const u32 tagl = cnt[2], tagh = cnt[3];
    const u32 tag32 = tagl | 0x80000000u;
    const u32 oy = WS_Y + (u32)(tid >> 2) * 4096u + (u32)b * 64u + (tid & 3) * 16, om = WS_ML + (u32)(tid >> 2) * 16u;
    u32x4 v0, v1, m0, m1;
    bool ok0 = false, ok1 = false;
    for (u32 spin = 0;; ++spin) {
      const u32x4 a0 = __builtin_amdgcn_raw_buffer_load_b128(rW, ok0 ? OOB : oy, 0, 16);
      const u32x4 a1 = __builtin_amdgcn_raw_buffer_load_b128(rW, ok1 ? OOB : oy, 128 * 4096, 16);
      const u32x4 b0 = __builtin_amdgcn_raw_buffer_load_b128(rW, ok0 ? OOB : om, 0, 16);
      const u32x4 b1 = __builtin_amdgcn_raw_buffer_load_b128(rW, ok1 ? OOB : om + 2048, 0, 16);
      if (!ok0) { v0 = a0; m0 = b0; ok0 = a0.y == tag32 && a0.w == tag32 && b0.y == tag32 && b0.w == tag32; }
      if (!ok1) { v1 = a1; m1 = b1; ok1 = a1.y == tag32 && a1.w == tag32 && b1.y == tag32 && b1.w == tag32; }
      if (__all(ok0 && ok1) || spin > SPIN_CAP) break;
      MEMBAR();
      __builtin_amdgcn_s_sleep(P_RSLEEP);
    }
    const bool slotB = ((tid >> 2) & 7) == 7;
    const float mm0 = slotB ? -INFINITY : uf(m0.x), mm1 = slotB ? -INFINITY : uf(m1.x);
    float M0 = mm0, M1 = mm1;
    M0 = fmaxf(M0, dpp<ROR4>(M0)); M1 = fmaxf(M1, dpp<ROR4>(M1));
    M0 = fmaxf(M0, dpp<ROR8>(M0)); M1 = fmaxf(M1, dpp<ROR8>(M1));
    M0 = x16_max(M0); M1 = x16_max(M1);
    const float w0 = ex2(mm0 - M0), w1 = ex2(mm1 - M1);
    float L0 = w0 * uf(m0.z), L1 = w1 * uf(m1.z);
    L0 += dpp<ROR4>(L0); L1 += dpp<ROR4>(L1);
    L0 += dpp<ROR8>(L0); L1 += dpp<ROR8>(L1);
    L0 = rs16(L0, L0); L1 = rs16(L1, L1);
    const float c0 = slotB ? 1.f / uf(m0.z) : w0 / L0, c1 = slotB ? 1.f / uf(m1.z) : w1 / L1;
    const float y0 = c0 * uf(v0.x) + c1 * uf(v1.x), y1 = c0 * uf(v0.z) + c1 * uf(v1.z);
    float ya0 = slotB ? 0.f : y0, ya1 = slotB ? 0.f : y1, yb0 = slotB ? y0 : 0.f, yb1 = slotB ? y1 : 0.f;
    float p0 = rs32(ya0, yb0), p1 = rs32(ya1, yb1);
    float y = rs16(p0, p1);
    y += dpp<ROR4>(y);
    y += dpp<ROR8>(y);
    if (c < 4) reinterpret_cast<float*>(lds + L_RED)[wave * 16 + (r >> 1) * 8 + 2 * c + (r & 1)] = y;
    WAIT_LDS();
    __builtin_amdgcn_s_barrier();
    if (tid < 8) {
      const f32x2* p = reinterpret_cast<const f32x2*>(lds + L_RED) + tid;
      f32x2 t = p[0];
#pragma unroll
      for (int w = 1; w < 8; ++w) t += p[8 * w];
      const int pc = 4 * b + (tid & 3);
      *reinterpret_cast<u32*>(out + 512 * (tid >> 2) + 128 * (pc >> 6) + 8 * (pc & 15) + 2 * ((pc >> 4) & 3)) = pk_bf16(t.x, t.y);
      if (b == 0 && tid == 0) __builtin_amdgcn_raw_buffer_store_b64(u32x2{tagl, tagh}, rW, WS_EPOCH, 0, 16);
    }
  }
}

static size_t ws_off(size_t ws_size) { return P_WSEND ? ((ws_size - 2 * (size_t)WS_END) & ~(size_t)4095) : 0; }

extern "C" void kernel_launch(void* const* d_in, const int* in_sizes, int n_in, void* d_out, int out_size, void* d_ws, size_t ws_size,
                              hipStream_t stream) {
  Args a;
  for (int br = 0; br < 2; ++br) {
    for (int i = 0; i < 5; ++i) a.p[br][i] = d_in[5 * br + i];
    a.p[br][5] = d_out; a.p[br][6] = (unsigned char*)d_ws + ws_off(ws_size); a.p[br][7] = nullptr;
  }
  hipLaunchKernelGGL(fused_decode, dim3(NWG), dim3(NTHR), 0, stream, a);
}
```

```cpp
#include <hip/hip_runtime.h>
#include <hip/hip_bf16.h>
#include <stdint.h>

typedef unsigned u32;
typedef unsigned long long u64;
typedef u32 u32x4 __attribute__((ext_vector_type(4)));
typedef u32 u32x2 __attribute__((ext_vector_type(2)));
typedef float f32x4 __attribute__((ext_vector_type(4)));
typedef float f32x2 __attribute__((ext_vector_type(2)));
typedef __bf16 bf16x2 __attribute__((ext_vector_type(2)));
typedef __bf16 bf16x8 __attribute__((ext_vector_type(8)));

#define P_U 2
#define P_NB 2
#define P_SPIN 20
#define P_DBG 0
#define P_DQ 16
#define P_HT 8
#define P_WSEND 0
#define P_RSLEEP 4
#define P_MIX 1

constexpr int DM = 512, DH = 128, SK = 32768, WIN = 4096;
constexpr int NWG = 256, NTHR = 512;
constexpr int NB = P_NB;
constexpr int DQ = P_DQ;
constexpr int HR = P_HT;
constexpr float SCALE = 0.08838834764831845f;
constexpr float LOG2E = 1.4426950408889634f;
constexpr int NBLK = SK / 16, BQ = NBLK / 7, BR = NBLK % 7;
constexpr int RMAX = (BQ + 1 + 3) / 4;

constexpr u32 WS_EPOCH = 0, WS_ML = 4096, WS_Y = 1048576, WS_END = 2097152, WS_DIAG = WS_END;
constexpr u32 OOB = 0x80000000u;
constexpr u32 SPIN_CAP = 1u << P_SPIN;

constexpr int L_QP = 0, L_AF = L_QP + 2048, AF_ROW = 272, L_ML = L_AF + 16 * AF_ROW, L_HM = L_ML + 64, L_RED = L_HM + 64, L_CNT = L_RED + 512,
              L_SC = L_CNT + 32, SC_W = (RMAX + NB) * 64, L_END = L_SC + 4 * SC_W;

#define SB() __builtin_amdgcn_sched_barrier(0)
#define MEMBAR() asm volatile("" ::: "memory")
#define WAIT_LDS() asm volatile("s_waitcnt lgkmcnt(0)" ::: "memory")
#define WAIT_VM() asm volatile("s_waitcnt vmcnt(0)" ::: "memory")

__device__ __forceinline__ float dot2(u32 a, u32 b, float c) {
  return __builtin_amdgcn_fdot2_f32_bf16(__builtin_bit_cast(bf16x2, a), __builtin_bit_cast(bf16x2, b), c, false);
}
template <int CTRL> __device__ __forceinline__ float dpp(float x) {
  return __builtin_bit_cast(float, __builtin_amdgcn_mov_dpp(__builtin_bit_cast(int, x), CTRL, 0xf, 0xf, true));
}
constexpr int ROR8 = 0x128, ROR4 = 0x124, ROR2 = 0x122, ROR1 = 0x121, QX2 = 0x4E  ;
__device__ __forceinline__ float blo(u32 w) { return __builtin_bit_cast(float, w << 16); }
__device__ __forceinline__ float bhi(u32 w) { return __builtin_bit_cast(float, w & 0xffff0000u); }
__device__ __forceinline__ float ex2(float x) { return __builtin_amdgcn_exp2f(x); }
__device__ __forceinline__ u32 fu(float x) { return __builtin_bit_cast(u32, x); }
__device__ __forceinline__ float uf(u32 x) { return __builtin_bit_cast(float, x); }
__device__ __forceinline__ u32 pk_bf16(float a, float b) {
  return __builtin_bit_cast(u32, __builtin_convertvector(f32x2{a, b}, bf16x2));
}
__device__ __forceinline__ float rs16(float a, float b) {
  auto s = __builtin_amdgcn_permlane16_swap(fu(a), fu(b), false, false);
  return uf(s[0]) + uf(s[1]);
}
__device__ __forceinline__ float rs32(float a, float b) {
  auto s = __builtin_amdgcn_permlane32_swap(fu(a), fu(b), false, false);
  return uf(s[0]) + uf(s[1]);
}
__device__ __forceinline__ float x16_max(float x) {
  auto s = __builtin_amdgcn_permlane16_swap(fu(x), fu(x), false, false);
  return fmaxf(uf(s[0]), uf(s[1]));
}
__device__ __forceinline__ float x32_max(float x) {
  auto s = __builtin_amdgcn_permlane32_swap(fu(x), fu(x), false, false);
  return fmaxf(uf(s[0]), uf(s[1]));
}
__device__ __forceinline__ void rscat8(const float (&v)[8], float& z0, float& z1) {
  const float w0 = rs32(v[0], v[4]), w1 = rs32(v[1], v[5]), w2 = rs32(v[2], v[6]), w3 = rs32(v[3], v[7]);
  z0 = rs16(w0, w2); z1 = rs16(w1, w3);
}

__device__ __forceinline__ f32x4 score_tile(const u32x4 (&K)[4], const u32x4 q, int nv) {
  float s[4];
#pragma unroll
  for (int u = 0; u < 4; ++u) {
    float d = dot2(K[u].x, q.x, 0.f);
    d = dot2(K[u].y, q.y, d); d = dot2(K[u].z, q.z, d); d = dot2(K[u].w, q.w, d);
    s[u] = d;
  }
#pragma unroll
  for (int u = 0; u < 4; ++u) s[u] += dpp<ROR8>(s[u]);
#pragma unroll
  for (int u = 0; u < 4; ++u) s[u] += dpp<ROR4>(s[u]);
#pragma unroll
  for (int u = 0; u < 4; ++u) s[u] += dpp<ROR2>(s[u]);
#pragma unroll
  for (int u = 0; u < 4; ++u) s[u] += dpp<ROR1>(s[u]);
#pragma unroll
  for (int u = 0; u < 4; u += 2) {
    const u32 r1 = pk_bf16(s[u], s[u + 1]);
    const u32 r2 = pk_bf16(blo(r1) * SCALE, bhi(r1) * SCALE);
    s[u] = blo(r2); s[u + 1] = bhi(r2);
  }
  return f32x4{s[0], nv > 1 ? s[1] : -INFINITY, nv > 2 ? s[2] : -INFINITY, nv > 3 ? s[3] : -INFINITY};
}
__device__ __forceinline__ void pv_tile(const u32x4 (&V)[4], const f32x4 s, float mL, float& l, float (&acc)[8]) {
#pragma unroll
  for (int u = 0; u < 4; u += 2) {
    const u32 pp = pk_bf16(ex2(fmaf(s[u], LOG2E, -mL)), ex2(fmaf(s[u + 1], LOG2E, -mL)));
    const u32 lo0 = pp & 0xffffu, hi0 = pp << 16, lo1 = pp >> 16, hi1 = pp & 0xffff0000u;
    l += uf(hi0) + uf(hi1);
#pragma unroll
    for (int k = 0; k < 4; ++k) {
      acc[2 * k] = dot2(V[u][k], lo0, acc[2 * k]);
      acc[2 * k + 1] = dot2(V[u][k], hi0, acc[2 * k + 1]);
    }
#pragma unroll
    for (int k = 0; k < 4; ++k) {
      acc[2 * k] = dot2(V[u + 1][k], lo1, acc[2 * k]);
      acc[2 * k + 1] = dot2(V[u + 1][k], hi1, acc[2 * k + 1]);
    }
  }
}
__device__ __forceinline__ float max4(const f32x4 s, float m) { return fmaxf(fmaxf(m, fmaxf(s.x, s.y)), fmaxf(s.z, s.w)); }

__device__ __forceinline__ void wave_merge(float& mL, float& l, float (&acc)[8], float& z0, float& z1) {
  const float mw = x32_max(x16_max(mL));
  const float sc = ex2(mL - mw);
  const float ls = l * sc;
  const float l1 = rs16(ls, ls);
  l = rs32(l1, l1);
  mL = mw;
#pragma unroll
  for (int k = 0; k < 8; ++k) acc[k] *= sc;
  rscat8(acc, z0, z1);
}

__device__ __forceinline__ void lds_arrive(u32* cnt, int lane) {
  MEMBAR();
  if (lane == 0) __hip_atomic_fetch_add(cnt, 1u, __ATOMIC_RELAXED, __HIP_MEMORY_SCOPE_WORKGROUP);
  MEMBAR();
}
__device__ __forceinline__ void lds_wait(u32* cnt, u32 n) {
  for (u32 spin = 0; spin < (1u << 24); ++spin) {
    if (__hip_atomic_load(cnt, __ATOMIC_RELAXED, __HIP_MEMORY_SCOPE_WORKGROUP) >= n) break;
    __builtin_amdgcn_s_sleep(1);
  }
  MEMBAR();
}

struct Args {
  const void* p[2][8];
};

#define STAMP(i)

__device__ __forceinline__ void park(unsigned char* lds, int wave, int lane, float mL, float l, float (&acc)[8]) {
  float z0, z1;
  wave_merge(mL, l, acc, z0, z1);
  const u32 hi = pk_bf16(z0, z1);
  const u32 lo = pk_bf16(z0 - blo(hi), z1 - bhi(hi));
  unsigned char* p = lds + L_AF + (2 * wave) * AF_ROW + 16 * (lane & 15) + 4 * (lane >> 4);
  *reinterpret_cast<u32*>(p) = hi;
  *reinterpret_cast<u32*>(p + AF_ROW) = lo;
  if (lane == 0) { reinterpret_cast<float*>(lds + L_ML)[wave] = mL; reinterpret_cast<float*>(lds + L_ML)[8 + wave] = l; }
}
__device__ __forceinline__ u32x4 read_q(const unsigned char* lds, int c) {
  f32x4 lo = *reinterpret_cast<const f32x4*>(lds + L_QP + 32 * c), hi = *reinterpret_cast<const f32x4*>(lds + L_QP + 32 * c + 16);
#pragma unroll
  for (int w = 1; w < 4; ++w) {
    lo += *reinterpret_cast<const f32x4*>(lds + L_QP + w * 512 + 32 * c);
    hi += *reinterpret_cast<const f32x4*>(lds + L_QP + w * 512 + 32 * c + 16);
  }
  return u32x4{pk_bf16(lo.x, lo.y), pk_bf16(lo.z, lo.w), pk_bf16(hi.x, hi.y), pk_bf16(hi.z, hi.w)};
}

__global__ void __launch_bounds__(NTHR, 2) fused_decode(Args a) {
  __shared__ __attribute__((aligned(16))) unsigned char lds[L_END];
  const int tid = threadIdx.x, lane = tid & 63, wave = __builtin_amdgcn_readfirstlane(tid >> 6);
  const int c = lane & 15, r = lane >> 4;
  const int b = blockIdx.x, xg = b & 7, j = b >> 3;
  const int h = 4 * xg + (j >> 3), s = j & 7;
  const int pidx = h * 8 + s;
  const bool isB = s == 7;
  u32* cnt = reinterpret_cast<u32*>(lds + L_CNT);
  const void* const* in = a.p[isB ? 1 : 0];
  const unsigned short* X = (const unsigned short*)in[0];
  const unsigned short* KC = (const unsigned short*)in[1];
  const unsigned short* VC = (const unsigned short*)in[2];
  const unsigned short* WQ = (const unsigned short*)in[3] + 128 * h;
  const unsigned short* WO = (const unsigned short*)in[4] + (size_t)128 * h * DM;
  unsigned short* const out = (unsigned short*)in[5];
  unsigned char* const ws = (unsigned char*)in[6];
  asm volatile("" :: "s"(X), "s"(KC), "s"(VC), "s"(WQ), "s"(WO), "s"(ws), "s"(out));
  const int nblk = isB ? WIN / 16 : BQ + (s < BR ? 1 : 0);
  const int R = (nblk + 3) >> 2;
  const size_t row0 = isB ? (size_t)(h >> 2) * SK + (SK - WIN) : (size_t)h * SK + (size_t)(s * BQ + (s < BR ? s : BR)) * 16;
  const auto rK = __builtin_amdgcn_make_buffer_rsrc((void*)(KC + row0 * DH), 0, (u32)nblk * 4096u, 0x00020000);
  const auto rV = __builtin_amdgcn_make_buffer_rsrc((void*)(VC + row0 * DH), 0, (u32)nblk * 4096u, 0x00020000);
  const auto rW = __builtin_amdgcn_make_buffer_rsrc((void*)ws, 0, WS_END, 0x00020000);
  const u32 vo0 = (u32)(wave & 3) * 1024u + lane * 16, vo1 = vo0 + 4096u, vo2 = vo0 + 8192u, vo3 = vo0 + 12288u;
  unsigned char* const sc = lds + L_SC + (wave & 3) * SC_W + 16 * r;

  if (wave < 4) {
    const auto rQ = __builtin_amdgcn_make_buffer_rsrc((void*)WQ, 0, (u32)(DM * 4096 - 128 * h) * 2u, 0x00020000);
    u32x4 xq[4], wq[32];
    u32 qoff[4];
    {
      const int rg = tid >> 4;
#pragma unroll
      for (int q = 0; q < 4; ++q) {
        const int rw = 32 * rg + 8 * ((q + s) & 3);
        xq[q] = __builtin_nontemporal_load(reinterpret_cast<const u32x4*>(X + rw));
        qoff[q] = (P_DBG & 1) ? OOB : (u32)rw * 8192u + 16u * c;
      }
    }
    auto ldq = [&](int i) { wq[i] = __builtin_amdgcn_raw_buffer_load_b128(rQ, qoff[i >> 3], (i & 7) * 8192, 2); };
    u32x4 tb[NB][4];
    const int TK = R - HR;
    auto issueK = [&](int bi, int t) {
      const u32 so = (u32)(t + HR) * 16384u;
      tb[bi][0] = __builtin_amdgcn_raw_buffer_load_b128(rK, vo0, so, 2); tb[bi][1] = __builtin_amdgcn_raw_buffer_load_b128(rK, vo1, so, 2);
      tb[bi][2] = __builtin_amdgcn_raw_buffer_load_b128(rK, vo2, so, 2); tb[bi][3] = __builtin_amdgcn_raw_buffer_load_b128(rK, vo3, so, 2);
    };
    auto issueV = [&](int bi, int t) {
      const u32 so = (u32)t * 16384u;
      tb[bi][0] = __builtin_amdgcn_raw_buffer_load_b128(rV, vo0, so, 2); tb[bi][1] = __builtin_amdgcn_raw_buffer_load_b128(rV, vo1, so, 2);
      tb[bi][2] = __builtin_amdgcn_raw_buffer_load_b128(rV, vo2, so, 2); tb[bi][3] = __builtin_amdgcn_raw_buffer_load_b128(rV, vo3, so, 2);
    };
#pragma unroll
    for (int i = 0; i < DQ; ++i) ldq(i);
    SB();
    __builtin_amdgcn_s_barrier();
    SB();
    {
      float qa[8];
#pragma unroll
      for (int k = 0; k < 8; ++k) qa[k] = 0.f;
#pragma unroll
      for (int i0 = 0; i0 < 32; i0 += 4) {
#pragma unroll
        for (int i = i0; i < i0 + 4; ++i) {
          const u32 xw = xq[i >> 3][(i & 7) >> 1];
          const u32 lo = (i & 1) ? xw >> 16 : xw & 0xffffu, hi = (i & 1) ? xw & 0xffff0000u : xw << 16;
#pragma unroll
          for (int k = 0; k < 4; ++k) { qa[2 * k] = dot2(wq[i][k], lo, qa[2 * k]); qa[2 * k + 1] = dot2(wq[i][k], hi, qa[2 * k + 1]); }
        }
        SB();
        if (i0 + DQ < 32) {
#pragma unroll
          for (int i = i0 + DQ; i < i0 + DQ + 4; ++i) ldq(i);
        }
        if (i0 + DQ == 32 || (DQ == 32 && i0 == 0)) {
#pragma unroll
          for (int bi = 0; bi < NB - 1; ++bi) issueK(bi, bi);
        }
        SB();
      }
      float z0, z1;
      rscat8(qa, z0, z1);
      *reinterpret_cast<f32x2*>(lds + L_QP + wave * 512 + 32 * c + 8 * r) = f32x2{z0, z1};
    }
    lds_arrive(cnt, lane);
    lds_wait(cnt, 4);
    const u32x4 q = read_q(lds, c);
    float m = -INFINITY;
    const int TKP = (TK + NB - 1) / NB * NB;
    int t0 = 0;
    do {
#pragma unroll
      for (int bi = 0; bi < NB; ++bi) {
        const int t = t0 + bi;
        issueK((bi + NB - 1) % NB, t + NB - 1);
        SB();
        const int nv = nblk - 4 * (t + HR);
        f32x4 sv = score_tile(tb[bi], q, nv);
        if (nv <= 0) sv.x = -INFINITY;
        m = max4(sv, m);
        if (c == 0) *reinterpret_cast<f32x4*>(sc + (t + HR) * 64) = sv;
        SB();
      }
      t0 += NB;
    } while (t0 < TKP - NB);
#pragma unroll
    for (int bi = 0; bi < NB; ++bi) {
      const int t = t0 + bi;
      if (bi == 0) issueK(NB - 1, t + NB - 1); else issueV(bi - 1, bi - 1);
      SB();
      const int nv = nblk - 4 * (t + HR);
      f32x4 sv = score_tile(tb[bi], q, nv);
      if (nv <= 0) sv.x = -INFINITY;
      m = max4(sv, m);
      if (c == 0) *reinterpret_cast<f32x4*>(sc + (t + HR) * 64) = sv;
      SB();
    }
    lds_wait(cnt + 1, 4);
    m = fmaxf(m, reinterpret_cast<const float*>(lds + L_HM)[wave * 4 + r]);
    float mL = m * LOG2E, l = 0.f, acc[8];
#pragma unroll
    for (int k = 0; k < 8; ++k) acc[k] = 0.f;
    t0 = 0;
    do {
#pragma unroll
      for (int bi = 0; bi < NB; ++bi) {
        const int t = t0 + bi;
        issueV((bi + NB - 1) % NB, t + NB - 1);
        const f32x4 sv = *reinterpret_cast<const f32x4*>(sc + t * 64);
        SB();
        if (bi == 0 || t < R) pv_tile(tb[bi], sv, mL, l, acc);
        SB();
      }
      t0 += NB;
    } while (t0 < R);
    park(lds, wave, lane, mL, l, acc);
    WAIT_LDS();
    __builtin_amdgcn_s_barrier();
  } else {
    const int hw = wave - 4, th = tid - 256;
    const u32x2 ep = __builtin_amdgcn_raw_buffer_load_b64(rW, WS_EPOCH, 0, 16);
    if (th < 8) cnt[th] = 0;
    WAIT_LDS();
    SB();
    __builtin_amdgcn_s_barrier();
    SB();
    {
      u32x4 kk[HR][4];
#pragma unroll
      for (int t = 0; t < HR; ++t) {
        kk[t][0] = __builtin_amdgcn_raw_buffer_load_b128(rK, vo0, t * 16384, 2); kk[t][1] = __builtin_amdgcn_raw_buffer_load_b128(rK, vo1, t * 16384, 2);
        kk[t][2] = __builtin_amdgcn_raw_buffer_load_b128(rK, vo2, t * 16384, 2); kk[t][3] = __builtin_amdgcn_raw_buffer_load_b128(rK, vo3, t * 16384, 2);
      }
      SB();
      lds_wait(cnt, 4);
      const u32x4 q = read_q(lds, c);
      float m = -INFINITY;
#pragma unroll
      for (int t = 0; t < HR; ++t) {
        const f32x4 sv = score_tile(kk[t], q, 4);
        m = max4(sv, m);
        if (c == 0) *reinterpret_cast<f32x4*>(sc + t * 64) = sv;
        SB();
      }
      if (c == 0) reinterpret_cast<float*>(lds + L_HM)[hw * 4 + r] = m;
      WAIT_LDS();
      lds_arrive(cnt + 1, lane);
    }
    SB();
    u32x4 wo[4][8];
    {
      const auto rO = __builtin_amdgcn_make_buffer_rsrc((void*)WO, 0, 128u * 1024u, 0x00020000);
      const u32 vo = (P_DBG & 2) ? OOB : (u32)r * 8192u + (u32)hw * 256u + 16u * c;
#pragma unroll
      for (int st = 0; st < 4; ++st)
#pragma unroll
        for (int i = 0; i < 8; ++i) wo[st][i] = __builtin_amdgcn_raw_buffer_load_b128(rO, vo + (i & 3) * 1024, (st * 32 + (i & 4)) * 1024, 2);
    }
    const u64 tag64 = (((u64)ep.y << 32) | ep.x) + 1;
    const u32 tag32 = (u32)tag64 | 0x80000000u;
    if (th == 0) { cnt[2] = (u32)tag64; cnt[3] = (u32)(tag64 >> 32); }
    SB();
    u32x4 wf[4][8];
#pragma unroll
    for (int st = 0; st < 4; ++st)
#pragma unroll
      for (int t = 0; t < 8; ++t)
#pragma unroll
        for (int k = 0; k < 4; ++k)
          wf[st][t][k] = __builtin_amdgcn_perm(wo[st][2 * k + 1][t >> 1], wo[st][2 * k][t >> 1], (t & 1) ? 0x07060302u : 0x05040100u);
    SB();
    WAIT_LDS();
    __builtin_amdgcn_s_barrier();
    {
      const f32x4 ma = *reinterpret_cast<const f32x4*>(lds + L_ML);
      const f32x2 mr = *reinterpret_cast<const f32x2*>(lds + L_ML + 8 * (r & 1)), lr = *reinterpret_cast<const f32x2*>(lds + L_ML + 32 + 8 * (r & 1));
      u32x4 af[4];
#pragma unroll
      for (int st = 0; st < 4; ++st) af[st] = *reinterpret_cast<const u32x4*>(lds + L_AF + (c & 7) * AF_ROW + st * 64 + 16 * r);
      const float M = fmaxf(fmaxf(ma.x, ma.y), fmaxf(ma.z, ma.w));
      const float w0 = r < 2 ? ex2(mr.x - M) : 0.f, w1 = r < 2 ? ex2(mr.y - M) : 0.f;
      float L = w0 * lr.x + w1 * lr.y;
      L = rs16(L, L);
      __builtin_amdgcn_raw_buffer_store_b128(u32x4{fu(M), tag32, fu(L), tag32}, rW, th == 0 ? WS_ML + (u32)pidx * 16u : OOB, 0, 16);
      float y[8];
#pragma unroll
      for (int t = 0; t < 8; ++t) {
        f32x4 d = {0.f, 0.f, 0.f, 0.f};
#pragma unroll
        for (int st = 0; st < 4; ++st)
          d = __builtin_amdgcn_mfma_f32_16x16x32_bf16(__builtin_bit_cast(bf16x8, af[st]), __builtin_bit_cast(bf16x8, wf[st][t]), d, 0, 0, 0);
        y[t] = w0 * (d.x + d.y) + w1 * (d.z + d.w);
      }
      float z0, z1;
      rscat8(y, z0, z1);
      __builtin_amdgcn_raw_buffer_store_b128(u32x4{fu(z0), tag32, fu(z1), tag32}, rW, WS_Y + (u32)pidx * 4096u + (u32)th * 16u, 0, 16);
    }
  }
  if (b < 64) {
    const u32 tagl = cnt[2], tagh = cnt[3];
    const u32 tag32 = tagl | 0x80000000u;
    const u32 oy = WS_Y + (u32)(tid >> 2) * 4096u + (u32)b * 64u + (tid & 3) * 16, om = WS_ML + (u32)(tid >> 2) * 16u;
    u32x4 v0, v1, m0, m1;
    bool ok0 = false, ok1 = false;
    for (u32 spin = 0;; ++spin) {
      const u32x4 a0 = __builtin_amdgcn_raw_buffer_load_b128(rW, ok0 ? OOB : oy, 0, 16);
      const u32x4 a1 = __builtin_amdgcn_raw_buffer_load_b128(rW, ok1 ? OOB : oy, 128 * 4096, 16);
      const u32x4 b0 = __builtin_amdgcn_raw_buffer_load_b128(rW, ok0 ? OOB : om, 0, 16);
      const u32x4 b1 = __builtin_amdgcn_raw_buffer_load_b128(rW, ok1 ? OOB : om + 2048, 0, 16);
      if (!ok0) { v0 = a0; m0 = b0; ok0 = a0.y == tag32 && a0.w == tag32 && b0.y == tag32 && b0.w == tag32; }
      if (!ok1) { v1 = a1; m1 = b1; ok1 = a1.y == tag32 && a1.w == tag32 && b1.y == tag32 && b1.w == tag32; }
      if (__all(ok0 && ok1) || spin > SPIN_CAP) break;
      MEMBAR();
      __builtin_amdgcn_s_sleep(P_RSLEEP);
    }
    const bool slotB = ((tid >> 2) & 7) == 7;
    const float mm0 = slotB ? -INFINITY : uf(m0.x), mm1 = slotB ? -INFINITY : uf(m1.x);
    float M0 = mm0, M1 = mm1;
    M0 = fmaxf(M0, dpp<ROR4>(M0)); M1 = fmaxf(M1, dpp<ROR4>(M1));
    M0 = fmaxf(M0, dpp<ROR8>(M0)); M1 = fmaxf(M1, dpp<ROR8>(M1));
    M0 = x16_max(M0); M1 = x16_max(M1);
    const float w0 = ex2(mm0 - M0), w1 = ex2(mm1 - M1);
    float L0 = w0 * uf(m0.z), L1 = w1 * uf(m1.z);
    L0 += dpp<ROR4>(L0); L1 += dpp<ROR4>(L1);
    L0 += dpp<ROR8>(L0); L1 += dpp<ROR8>(L1);
    L0 = rs16(L0, L0); L1 = rs16(L1, L1);
    const float c0 = slotB ? 1.f / uf(m0.z) : w0 / L0, c1 = slotB ? 1.f / uf(m1.z) : w1 / L1;
    const float y0 = c0 * uf(v0.x) + c1 * uf(v1.x), y1 = c0 * uf(v0.z) + c1 * uf(v1.z);
    float ya0 = slotB ? 0.f : y0, ya1 = slotB ? 0.f : y1, yb0 = slotB ? y0 : 0.f, yb1 = slotB ? y1 : 0.f;
    float p0 = rs32(ya0, yb0), p1 = rs32(ya1, yb1);
    float y = rs16(p0, p1);
    y += dpp<ROR4>(y);
    y += dpp<ROR8>(y);
    if (c < 4) reinterpret_cast<float*>(lds + L_RED)[wave * 16 + (r >> 1) * 8 + 2 * c + (r & 1)] = y;
    WAIT_LDS();
    __builtin_amdgcn_s_barrier();
    if (tid < 8) {
      const f32x2* p = reinterpret_cast<const f32x2*>(lds + L_RED) + tid;
      f32x2 t = p[0];
#pragma unroll
      for (int w = 1; w < 8; ++w) t += p[8 * w];
      const int pc = 4 * b + (tid & 3);
      *reinterpret_cast<u32*>(out + 512 * (tid >> 2) + 128 * (pc >> 6) + 8 * (pc & 15) + 2 * ((pc >> 4) & 3)) = pk_bf16(t.x, t.y);
      if (b == 0 && tid == 0) __builtin_amdgcn_raw_buffer_store_b64(u32x2{tagl, tagh}, rW, WS_EPOCH, 0, 16);
    }
  }
}

static size_t ws_off(size_t ws_size) { return P_WSEND ? ((ws_size - 2 * (size_t)WS_END) & ~(size_t)4095) : 0; }

extern "C" void kernel_launch(void* const* d_in, const int* in_sizes, int n_in, void* d_out, int out_size, void* d_ws, size_t ws_size,
                              hipStream_t stream) {
  Args a;
  for (int br = 0; br < 2; ++br) {
    for (int i = 0; i < 5; ++i) a.p[br][i] = d_in[5 * br + i];
    a.p[br][5] = d_out; a.p[br][6] = (unsigned char*)d_ws + ws_off(ws_size); a.p[br][7] = nullptr;
  }
  hipLaunchKernelGGL(fused_decode, dim3(NWG), dim3(NTHR), 0, stream, a);
}
```

```cpp
#include <hip/hip_runtime.h>
#include <hip/hip_bf16.h>
#include <stdint.h>

typedef unsigned u32;
typedef unsigned long long u64;
typedef u32 u32x4 __attribute__((ext_vector_type(4)));
typedef u32 u32x2 __attribute__((ext_vector_type(2)));
typedef float f32x4 __attribute__((ext_vector_type(4)));
typedef float f32x2 __attribute__((ext_vector_type(2)));
typedef __bf16 bf16x2 __attribute__((ext_vector_type(2)));
typedef __bf16 bf16x8 __attribute__((ext_vector_type(8)));

#define P_U 2
#define P_NB 2
#define P_SPIN 20
#define P_DBG 0
#define P_DQ 16
#define P_HT 8
#define P_WSEND 0
#define P_RSLEEP 4
#define P_TA 400
#define P_TK 4050
#define P_TV 7700
#define P_DH 50
#define P_TW 0
#define P_DW 100
#define P_PB 1
#define P_MIX 1

constexpr int DM = 512, DH = 128, SK = 32768, WIN = 4096;
constexpr int NWG = 256, NTHR = 512;
constexpr int NB = P_NB;
constexpr int DQ = P_DQ;
constexpr int HR = P_HT;
constexpr float SCALE = 0.08838834764831845f;
constexpr float LOG2E = 1.4426950408889634f;
constexpr int NBLK = SK / 16, BQ = NBLK / 7, BR = NBLK % 7;
constexpr int RMAX = (BQ + 1 + 3) / 4;

constexpr u32 WS_EPOCH = 0, WS_ML = 4096, WS_Y = 1048576, WS_END = 2097152, WS_DIAG = WS_END;
constexpr u32 OOB = 0x80000000u;
constexpr u32 SPIN_CAP = 1u << P_SPIN;

constexpr int L_QP = 0, L_AF = L_QP + 2048, AF_ROW = 272, L_ML = L_AF + 16 * AF_ROW, L_HM = L_ML + 64, L_RED = L_HM + 64, L_CNT = L_RED + 512,
              L_SC = L_CNT + 32, SC_W = (RMAX + NB) * 64, L_END = L_SC + 4 * SC_W;

#define SB() __builtin_amdgcn_sched_barrier(0)
#define MEMBAR() asm volatile("" ::: "memory")
#define WAIT_LDS() asm volatile("s_waitcnt lgkmcnt(0)" ::: "memory")
#define WAIT_VM() asm volatile("s_waitcnt vmcnt(0)" ::: "memory")

__device__ __forceinline__ float dot2(u32 a, u32 b, float c) {
  return __builtin_amdgcn_fdot2_f32_bf16(__builtin_bit_cast(bf16x2, a), __builtin_bit_cast(bf16x2, b), c, false);
}
template <int CTRL> __device__ __forceinline__ float dpp(float x) {
  return __builtin_bit_cast(float, __builtin_amdgcn_mov_dpp(__builtin_bit_cast(int, x), CTRL, 0xf, 0xf, true));
}
constexpr int ROR8 = 0x128, ROR4 = 0x124, ROR2 = 0x122, ROR1 = 0x121, QX2 = 0x4E  ;
__device__ __forceinline__ float blo(u32 w) { return __builtin_bit_cast(float, w << 16); }
__device__ __forceinline__ float bhi(u32 w) { return __builtin_bit_cast(float, w & 0xffff0000u); }
__device__ __forceinline__ float ex2(float x) { return __builtin_amdgcn_exp2f(x); }
__device__ __forceinline__ u32 fu(float x) { return __builtin_bit_cast(u32, x); }
__device__ __forceinline__ float uf(u32 x) { return __builtin_bit_cast(float, x); }
__device__ __forceinline__ u32 pk_bf16(float a, float b) {
  return __builtin_bit_cast(u32, __builtin_convertvector(f32x2{a, b}, bf16x2));
}
__device__ __forceinline__ float rs16(float a, float b) {
  auto s = __builtin_amdgcn_permlane16_swap(fu(a), fu(b), false, false);
  return uf(s[0]) + uf(s[1]);
}
__device__ __forceinline__ float rs32(float a, float b) {
  auto s = __builtin_amdgcn_permlane32_swap(fu(a), fu(b), false, false);
  return uf(s[0]) + uf(s[1]);
}
__device__ __forceinline__ float x16_max(float x) {
  auto s = __builtin_amdgcn_permlane16_swap(fu(x), fu(x), false, false);
  return fmaxf(uf(s[0]), uf(s[1]));
}
__device__ __forceinline__ float x32_max(float x) {
  auto s = __builtin_amdgcn_permlane32_swap(fu(x), fu(x), false, false);
  return fmaxf(uf(s[0]), uf(s[1]));
}
__device__ __forceinline__ void rscat8(const float (&v)[8], float& z0, float& z1) {
  const float w0 = rs32(v[0], v[4]), w1 = rs32(v[1], v[5]), w2 = rs32(v[2], v[6]), w3 = rs32(v[3], v[7]);
  z0 = rs16(w0, w2); z1 = rs16(w1, w3);
}

__device__ __forceinline__ f32x4 score_tile(const u32x4 (&K)[4], const u32x4 q, int nv) {
  float s[4];
#pragma unroll
  for (int u = 0; u < 4; ++u) {
    float d = dot2(K[u].x, q.x, 0.f);
    d = dot2(K[u].y, q.y, d); d = dot2(K[u].z, q.z, d); d = dot2(K[u].w, q.w, d);
    s[u] = d;
  }
#pragma unroll
  for (int u = 0; u < 4; ++u) s[u] += dpp<ROR8>(s[u]);
#pragma unroll
  for (int u = 0; u < 4; ++u) s[u] += dpp<ROR4>(s[u]);
#pragma unroll
  for (int u = 0; u < 4; ++u) s[u] += dpp<ROR2>(s[u]);
#pragma unroll
  for (int u = 0; u < 4; ++u) s[u] += dpp<ROR1>(s[u]);
#pragma unroll
  for (int u = 0; u < 4; u += 2) {
    const u32 r1 = pk_bf16(s[u], s[u + 1]);
    const u32 r2 = pk_bf16(blo(r1) * SCALE, bhi(r1) * SCALE);
    s[u] = blo(r2); s[u + 1] = bhi(r2);
  }
  return f32x4{s[0], nv > 1 ? s[1] : -INFINITY, nv > 2 ? s[2] : -INFINITY, nv > 3 ? s[3] : -INFINITY};
}
__device__ __forceinline__ void pv_tile(const u32x4 (&V)[4], const f32x4 s, float mL, float& l, float (&acc)[8]) {
#pragma unroll
  for (int u = 0; u < 4; u += 2) {
    const u32 pp = pk_bf16(ex2(fmaf(s[u], LOG2E, -mL)), ex2(fmaf(s[u + 1], LOG2E, -mL)));
    const u32 lo0 = pp & 0xffffu, hi0 = pp << 16, lo1 = pp >> 16, hi1 = pp & 0xffff0000u;
    l += uf(hi0) + uf(hi1);
#pragma unroll
    for (int k = 0; k < 4; ++k) {
      acc[2 * k] = dot2(V[u][k], lo0, acc[2 * k]);
      acc[2 * k + 1] = dot2(V[u][k], hi0, acc[2 * k + 1]);
    }
#pragma unroll
    for (int k = 0; k < 4; ++k) {
      acc[2 * k] = dot2(V[u + 1][k], lo1, acc[2 * k]);
      acc[2 * k + 1] = dot2(V[u + 1][k], hi1, acc[2 * k + 1]);
    }
  }
}
__device__ __forceinline__ float max4(const f32x4 s, float m) { return fmaxf(fmaxf(m, fmaxf(s.x, s.y)), fmaxf(s.z, s.w)); }

__device__ __forceinline__ void wave_merge(float& mL, float& l, float (&acc)[8], float& z0, float& z1) {
  const float mw = x32_max(x16_max(mL));
  const float sc = ex2(mL - mw);
  const float ls = l * sc;
  const float l1 = rs16(ls, ls);
  l = rs32(l1, l1);
  mL = mw;
#pragma unroll
  for (int k = 0; k < 8; ++k) acc[k] *= sc;
  rscat8(acc, z0, z1);
}

__device__ __forceinline__ void lds_arrive(u32* cnt, int lane) {
  MEMBAR();
  if (lane == 0) __hip_atomic_fetch_add(cnt, 1u, __ATOMIC_RELAXED, __HIP_MEMORY_SCOPE_WORKGROUP);
  MEMBAR();
}
__device__ __forceinline__ void lds_wait(u32* cnt, u32 n) {
  for (u32 spin = 0; spin < (1u << 24); ++spin) {
    if (__hip_atomic_load(cnt, __ATOMIC_RELAXED, __HIP_MEMORY_SCOPE_WORKGROUP) >= n) break;
    __builtin_amdgcn_s_sleep(1);
  }
  MEMBAR();
}

__device__ __forceinline__ void wait_until(u32 tbase, u32 t) {
  for (int it = 0; it < 4096; ++it) {
    if ((u32)wall_clock64() - tbase >= t) break;
    __builtin_amdgcn_s_sleep(1);
  }
}

struct Args {
  const void* p[2][8];
};

#define STAMP(i)

__device__ __forceinline__ void park(unsigned char* lds, int wave, int lane, float mL, float l, float (&acc)[8]) {
  float z0, z1;
  wave_merge(mL, l, acc, z0, z1);
  const u32 hi = pk_bf16(z0, z1);
  const u32 lo = pk_bf16(z0 - blo(hi), z1 - bhi(hi));
  unsigned char* p = lds + L_AF + (2 * wave) * AF_ROW + 16 * (lane & 15) + 4 * (lane >> 4);
  *reinterpret_cast<u32*>(p) = hi;
  *reinterpret_cast<u32*>(p + AF_ROW) = lo;
  if (lane == 0) { reinterpret_cast<float*>(lds + L_ML)[wave] = mL; reinterpret_cast<float*>(lds + L_ML)[8 + wave] = l; }
}
__device__ __forceinline__ u32x4 read_q(const unsigned char* lds, int c) {
  f32x4 lo = *reinterpret_cast<const f32x4*>(lds + L_QP + 32 * c), hi = *reinterpret_cast<const f32x4*>(lds + L_QP + 32 * c + 16);
#pragma unroll
  for (int w = 1; w < 4; ++w) {
    lo += *reinterpret_cast<const f32x4*>(lds + L_QP + w * 512 + 32 * c);
    hi += *reinterpret_cast<const f32x4*>(lds + L_QP + w * 512 + 32 * c + 16);
  }
  return u32x4{pk_bf16(lo.x, lo.y), pk_bf16(lo.z, lo.w), pk_bf16(hi.x, hi.y), pk_bf16(hi.z, hi.w)};
}

__global__ void __launch_bounds__(NTHR, 2) fused_decode(Args a) {
  __shared__ __attribute__((aligned(16))) unsigned char lds[L_END];
  const int tid = threadIdx.x, lane = tid & 63, wave = __builtin_amdgcn_readfirstlane(tid >> 6);
  const int c = lane & 15, r = lane >> 4;
  const int b = blockIdx.x, xg = b & 7, j = b >> 3;
  const int h = 4 * xg + (j >> 3), s = j & 7;
  const int pidx = h * 8 + s;
  const bool isB = s == 7;
  u32* cnt = reinterpret_cast<u32*>(lds + L_CNT);
  const u32 tbase = (u32)wall_clock64();
  const void* const* in = a.p[isB ? 1 : 0];
  const unsigned short* X = (const unsigned short*)in[0];
  const unsigned short* KC = (const unsigned short*)in[1];
  const unsigned short* VC = (const unsigned short*)in[2];
  const unsigned short* WQ = (const unsigned short*)in[3] + 128 * h;
  const unsigned short* WO = (const unsigned short*)in[4] + (size_t)128 * h * DM;
  unsigned short* const out = (unsigned short*)in[5];
  unsigned char* const ws = (unsigned char*)in[6];
  asm volatile("" :: "s"(X), "s"(KC), "s"(VC), "s"(WQ), "s"(WO), "s"(ws), "s"(out));
  const int nblk = isB ? WIN / 16 : BQ + (s < BR ? 1 : 0);
  const int R = (nblk + 3) >> 2;
  const size_t row0 = isB ? (size_t)(h >> 2) * SK + (SK - WIN) : (size_t)h * SK + (size_t)(s * BQ + (s < BR ? s : BR)) * 16;
  const auto rK = __builtin_amdgcn_make_buffer_rsrc((void*)(KC + row0 * DH), 0, (u32)nblk * 4096u, 0x00020000);
  const auto rV = __builtin_amdgcn_make_buffer_rsrc((void*)(VC + row0 * DH), 0, (u32)nblk * 4096u, 0x00020000);
  const auto rW = __builtin_amdgcn_make_buffer_rsrc((void*)ws, 0, WS_END, 0x00020000);
  const u32 vo0 = (u32)(wave & 3) * 1024u + lane * 16, vo1 = vo0 + 4096u, vo2 = vo0 + 8192u, vo3 = vo0 + 12288u;
  unsigned char* const sc = lds + L_SC + (wave & 3) * SC_W + 16 * r;

  if (wave < 4) {
    const auto rQ = __builtin_amdgcn_make_buffer_rsrc((void*)WQ, 0, (u32)(DM * 4096 - 128 * h) * 2u, 0x00020000);
    u32x4 xq[4], wq[32];
    u32 qoff[4];
    {
      const int rg = tid >> 4;
#pragma unroll
      for (int q = 0; q < 4; ++q) {
        const int rw = 32 * rg + 8 * ((q + s) & 3);
        xq[q] = __builtin_nontemporal_load(reinterpret_cast<const u32x4*>(X + rw));
        qoff[q] = (P_DBG & 1) ? OOB : (u32)rw * 8192u + 16u * c;
      }
    }
    auto ldq = [&](int i) { wq[i] = __builtin_amdgcn_raw_buffer_load_b128(rQ, qoff[i >> 3], (i & 7) * 8192, 2); };
    u32x4 tb[NB][4];
    const int TK = R - HR;
    auto issueK = [&](int bi, int t) {
      const u32 so = (u32)(t + HR) * 16384u;
      tb[bi][0] = __builtin_amdgcn_raw_buffer_load_b128(rK, vo0, so, 2); tb[bi][1] = __builtin_amdgcn_raw_buffer_load_b128(rK, vo1, so, 2);
      tb[bi][2] = __builtin_amdgcn_raw_buffer_load_b128(rK, vo2, so, 2); tb[bi][3] = __builtin_amdgcn_raw_buffer_load_b128(rK, vo3, so, 2);
    };
    auto issueV = [&](int bi, int t) {
      const u32 so = (u32)t * 16384u;
      tb[bi][0] = __builtin_amdgcn_raw_buffer_load_b128(rV, vo0, so, 2); tb[bi][1] = __builtin_amdgcn_raw_buffer_load_b128(rV, vo1, so, 2);
      tb[bi][2] = __builtin_amdgcn_raw_buffer_load_b128(rV, vo2, so, 2); tb[bi][3] = __builtin_amdgcn_raw_buffer_load_b128(rV, vo3, so, 2);
    };
#pragma unroll
    for (int i = 0; i < DQ; ++i) ldq(i);
    SB();
    __builtin_amdgcn_s_barrier();
    SB();
    {
      float qa[8];
#pragma unroll
      for (int k = 0; k < 8; ++k) qa[k] = 0.f;
#pragma unroll
      for (int i0 = 0; i0 < 32; i0 += 4) {
#pragma unroll
        for (int i = i0; i < i0 + 4; ++i) {
          const u32 xw = xq[i >> 3][(i & 7) >> 1];
          const u32 lo = (i & 1) ? xw >> 16 : xw & 0xffffu, hi = (i & 1) ? xw & 0xffff0000u : xw << 16;
#pragma unroll
          for (int k = 0; k < 4; ++k) { qa[2 * k] = dot2(wq[i][k], lo, qa[2 * k]); qa[2 * k + 1] = dot2(wq[i][k], hi, qa[2 * k + 1]); }
        }
        SB();
        if (i0 + DQ < 32) {
#pragma unroll
          for (int i = i0 + DQ; i < i0 + DQ + 4; ++i) ldq(i);
        }
        if (i0 + DQ == 32 || (DQ == 32 && i0 == 0)) {
#pragma unroll
          for (int bi = 0; bi < NB - 1; ++bi) issueK(bi, bi);
        }
        SB();
      }
      float z0, z1;
      rscat8(qa, z0, z1);
      *reinterpret_cast<f32x2*>(lds + L_QP + wave * 512 + 32 * c + 8 * r) = f32x2{z0, z1};
    }
    lds_arrive(cnt, lane);
    lds_wait(cnt, 4);
    const u32x4 q = read_q(lds, c);
    u32 dl = P_TA * 256u;
    u32 dstep = isB ? (P_PB ? (P_TK - P_TA) * 256u / (WIN / 64 - HR) : 0u) : (P_TK - P_TA) * 256u / (RMAX - HR);
    auto pace = [&]() {
      dl += dstep;
      wait_until(tbase, dl >> 8);
    };
    float m = -INFINITY;
    const int TKP = (TK + NB - 1) / NB * NB;
    int t0 = 0;
    do {
#pragma unroll
      for (int bi = 0; bi < NB; ++bi) {
        const int t = t0 + bi;
        pace();
        issueK((bi + NB - 1) % NB, t + NB - 1);
        SB();
        const int nv = nblk - 4 * (t + HR);
        f32x4 sv = score_tile(tb[bi], q, nv);
        if (nv <= 0) sv.x = -INFINITY;
        m = max4(sv, m);
        if (c == 0) *reinterpret_cast<f32x4*>(sc + (t + HR) * 64) = sv;
        SB();
      }
      t0 += NB;
    } while (t0 < TKP - NB);
#pragma unroll
    for (int bi = 0; bi < NB; ++bi) {
      const int t = t0 + bi;
      pace();
      if (bi == 0) issueK(NB - 1, t + NB - 1); else issueV(bi - 1, bi - 1);
      SB();
      const int nv = nblk - 4 * (t + HR);
      f32x4 sv = score_tile(tb[bi], q, nv);
      if (nv <= 0) sv.x = -INFINITY;
      m = max4(sv, m);
      if (c == 0) *reinterpret_cast<f32x4*>(sc + (t + HR) * 64) = sv;
      SB();
    }
    lds_wait(cnt + 1, 4);
    m = fmaxf(m, reinterpret_cast<const float*>(lds + L_HM)[wave * 4 + r]);
    float mL = m * LOG2E, l = 0.f, acc[8];
#pragma unroll
    for (int k = 0; k < 8; ++k) acc[k] = 0.f;
    dstep = isB ? (P_PB ? (P_TV - P_TK) * 256u / (WIN / 64) : 0u) : (P_TV - P_TK) * 256u / RMAX;
    t0 = 0;
    do {
#pragma unroll
      for (int bi = 0; bi < NB; ++bi) {
        const int t = t0 + bi;
        if (t + NB - 1 < R) pace();
        issueV((bi + NB - 1) % NB, t + NB - 1);
        const f32x4 sv = *reinterpret_cast<const f32x4*>(sc + t * 64);
        SB();
        if (bi == 0 || t < R) pv_tile(tb[bi], sv, mL, l, acc);
        SB();
      }
      t0 += NB;
    } while (t0 < R);
    park(lds, wave, lane, mL, l, acc);
    WAIT_LDS();
    __builtin_amdgcn_s_barrier();
  } else {
    const int hw = wave - 4, th = tid - 256;
    const u32x2 ep = __builtin_amdgcn_raw_buffer_load_b64(rW, WS_EPOCH, 0, 16);
    if (th < 8) cnt[th] = 0;
    WAIT_LDS();
    SB();
    __builtin_amdgcn_s_barrier();
    SB();
    {
      u32x4 kk[HR][4];
#pragma unroll
      for (int t = 0; t < HR; ++t) {
        if (P_DH && t) { wait_until(tbase, t * P_DH); SB(); }
        kk[t][0] = __builtin_amdgcn_raw_buffer_load_b128(rK, vo0, t * 16384, 2); kk[t][1] = __builtin_amdgcn_raw_buffer_load_b128(rK, vo1, t * 16384, 2);
        kk[t][2] = __builtin_amdgcn_raw_buffer_load_b128(rK, vo2, t * 16384, 2); kk[t][3] = __builtin_amdgcn_raw_buffer_load_b128(rK, vo3, t * 16384, 2);
      }
      SB();
      lds_wait(cnt, 4);
      const u32x4 q = read_q(lds, c);
      float m = -INFINITY;
#pragma unroll
      for (int t = 0; t < HR; ++t) {
        const f32x4 sv = score_tile(kk[t], q, 4);
        m = max4(sv, m);
        if (c == 0) *reinterpret_cast<f32x4*>(sc + t * 64) = sv;
        SB();
      }
      if (c == 0) reinterpret_cast<float*>(lds + L_HM)[hw * 4 + r] = m;
      WAIT_LDS();
      lds_arrive(cnt + 1, lane);
    }
    SB();
    u32x4 wo[4][8];
    {
      const auto rO = __builtin_amdgcn_make_buffer_rsrc((void*)WO, 0, 128u * 1024u, 0x00020000);
      const u32 vo = (P_DBG & 2) ? OOB : (u32)r * 8192u + (u32)hw * 256u + 16u * c;
#pragma unroll
      for (int st = 0; st < 4; ++st)
#pragma unroll
        for (int i = 0; i < 8; ++i) {
          if (P_TW && (i & 3) == 0) { SB(); wait_until(tbase, P_TW + (2 * st + (i >> 2)) * P_DW); SB(); }
          wo[st][i] = __builtin_amdgcn_raw_buffer_load_b128(rO, vo + (i & 3) * 1024, (st * 32 + (i & 4)) * 1024, 2);
        }
    }
    const u64 tag64 = (((u64)ep.y << 32) | ep.x) + 1;
    const u32 tag32 = (u32)tag64 | 0x80000000u;
    if (th == 0) { cnt[2] = (u32)tag64; cnt[3] = (u32)(tag64 >> 32); }
    SB();
    u32x4 wf[4][8];
#pragma unroll
    for (int st = 0; st < 4; ++st)
#pragma unroll
      for (int t = 0; t < 8; ++t)
#pragma unroll
        for (int k = 0; k < 4; ++k)
          wf[st][t][k] = __builtin_amdgcn_perm(wo[st][2 * k + 1][t >> 1], wo[st][2 * k][t >> 1], (t & 1) ? 0x07060302u : 0x05040100u);
    SB();
    WAIT_LDS();
    __builtin_amdgcn_s_barrier();
    {
      const f32x4 ma = *reinterpret_cast<const f32x4*>(lds + L_ML);
      const f32x2 mr = *reinterpret_cast<const f32x2*>(lds + L_ML + 8 * (r & 1)), lr = *reinterpret_cast<const f32x2*>(lds + L_ML + 32 + 8 * (r & 1));
      u32x4 af[4];
#pragma unroll
      for (int st = 0; st < 4; ++st) af[st] = *reinterpret_cast<const u32x4*>(lds + L_AF + (c & 7) * AF_ROW + st * 64 + 16 * r);
      const float M = fmaxf(fmaxf(ma.x, ma.y), fmaxf(ma.z, ma.w));
      const float w0 = r < 2 ? ex2(mr.x - M) : 0.f, w1 = r < 2 ? ex2(mr.y - M) : 0.f;
      float L = w0 * lr.x + w1 * lr.y;
      L = rs16(L, L);
      __builtin_amdgcn_raw_buffer_store_b128(u32x4{fu(M), tag32, fu(L), tag32}, rW, th == 0 ? WS_ML + (u32)pidx * 16u : OOB, 0, 16);
      float y[8];
#pragma unroll
      for (int t = 0; t < 8; ++t) {
        f32x4 d = {0.f, 0.f, 0.f, 0.f};
#pragma unroll
        for (int st = 0; st < 4; ++st)
          d = __builtin_amdgcn_mfma_f32_16x16x32_bf16(__builtin_bit_cast(bf16x8, af[st]), __builtin_bit_cast(bf16x8, wf[st][t]), d, 0, 0, 0);
        y[t] = w0 * (d.x + d.y) + w1 * (d.z + d.w);
      }
      float z0, z1;
      rscat8(y, z0, z1);
      __builtin_amdgcn_raw_buffer_store_b128(u32x4{fu(z0), tag32, fu(z1), tag32}, rW, WS_Y + (u32)pidx * 4096u + (u32)th * 16u, 0, 16);
    }
  }
  if (b < 64) {
    const u32 tagl = cnt[2], tagh = cnt[3];
    const u32 tag32 = tagl | 0x80000000u;
    const u32 oy = WS_Y + (u32)(tid >> 2) * 4096u + (u32)b * 64u + (tid & 3) * 16, om = WS_ML + (u32)(tid >> 2) * 16u;
    u32x4 v0, v1, m0, m1;
    bool ok0 = false, ok1 = false;
    for (u32 spin = 0;; ++spin) {
      const u32x4 a0 = __builtin_amdgcn_raw_buffer_load_b128(rW, ok0 ? OOB : oy, 0, 16);
      const u32x4 a1 = __builtin_amdgcn_raw_buffer_load_b128(rW, ok1 ? OOB : oy, 128 * 4096, 16);
      const u32x4 b0 = __builtin_amdgcn_raw_buffer_load_b128(rW, ok0 ? OOB : om, 0, 16);
      const u32x4 b1 = __builtin_amdgcn_raw_buffer_load_b128(rW, ok1 ? OOB : om + 2048, 0, 16);
      if (!ok0) { v0 = a0; m0 = b0; ok0 = a0.y == tag32 && a0.w == tag32 && b0.y == tag32 && b0.w == tag32; }
      if (!ok1) { v1 = a1; m1 = b1; ok1 = a1.y == tag32 && a1.w == tag32 && b1.y == tag32 && b1.w == tag32; }
      if (__all(ok0 && ok1) || spin > SPIN_CAP) break;
      MEMBAR();
      __builtin_amdgcn_s_sleep(P_RSLEEP);
    }
    const bool slotB = ((tid >> 2) & 7) == 7;
    const float mm0 = slotB ? -INFINITY : uf(m0.x), mm1 = slotB ? -INFINITY : uf(m1.x);
    float M0 = mm0, M1 = mm1;
    M0 = fmaxf(M0, dpp<ROR4>(M0)); M1 = fmaxf(M1, dpp<ROR4>(M1));
    M0 = fmaxf(M0, dpp<ROR8>(M0)); M1 = fmaxf(M1, dpp<ROR8>(M1));
    M0 = x16_max(M0); M1 = x16_max(M1);
    const float w0 = ex2(mm0 - M0), w1 = ex2(mm1 - M1);
    float L0 = w0 * uf(m0.z), L1 = w1 * uf(m1.z);
    L0 += dpp<ROR4>(L0); L1 += dpp<ROR4>(L1);
    L0 += dpp<ROR8>(L0); L1 += dpp<ROR8>(L1);
    L0 = rs16(L0, L0); L1 = rs16(L1, L1);
    const float c0 = slotB ? 1.f / uf(m0.z) : w0 / L0, c1 = slotB ? 1.f / uf(m1.z) : w1 / L1;
    const float y0 = c0 * uf(v0.x) + c1 * uf(v1.x), y1 = c0 * uf(v0.z) + c1 * uf(v1.z);
    float ya0 = slotB ? 0.f : y0, ya1 = slotB ? 0.f : y1, yb0 = slotB ? y0 : 0.f, yb1 = slotB ? y1 : 0.f;
    float p0 = rs32(ya0, yb0), p1 = rs32(ya1, yb1);
    float y = rs16(p0, p1);
    y += dpp<ROR4>(y);
    y += dpp<ROR8>(y);
    if (c < 4) reinterpret_cast<float*>(lds + L_RED)[wave * 16 + (r >> 1) * 8 + 2 * c + (r & 1)] = y;
    WAIT_LDS();
    __builtin_amdgcn_s_barrier();
    if (tid < 8) {
      const f32x2* p = reinterpret_cast<const f32x2*>(lds + L_RED) + tid;
      f32x2 t = p[0];
#pragma unroll
      for (int w = 1; w < 8; ++w) t += p[8 * w];
      const int pc = 4 * b + (tid & 3);
      *reinterpret_cast<u32*>(out + 512 * (tid >> 2) + 128 * (pc >> 6) + 8 * (pc & 15) + 2 * ((pc >> 4) & 3)) = pk_bf16(t.x, t.y);
      if (b == 0 && tid == 0) __builtin_amdgcn_raw_buffer_store_b64(u32x2{tagl, tagh}, rW, WS_EPOCH, 0, 16);
    }
  }
}

static size_t ws_off(size_t ws_size) { return P_WSEND ? ((ws_size - 2 * (size_t)WS_END) & ~(size_t)4095) : 0; }

extern "C" void kernel_launch(void* const* d_in, const int* in_sizes, int n_in, void* d_out, int out_size, void* d_ws, size_t ws_size,
                              hipStream_t stream) {
  Args a;
  for (int br = 0; br < 2; ++br) {
    for (int i = 0; i < 5; ++i) a.p[br][i] = d_in[5 * br + i];
    a.p[br][5] = d_out; a.p[br][6] = (unsigned char*)d_ws + ws_off(ws_size); a.p[br][7] = nullptr;
  }
  hipLaunchKernelGGL(fused_decode, dim3(NWG), dim3(NTHR), 0, stream, a);
}
```

```cpp
#include <hip/hip_runtime.h>
#include <hip/hip_bf16.h>
#include <stdint.h>

typedef unsigned u32;
typedef unsigned long long u64;
typedef u32 u32x4 __attribute__((ext_vector_type(4)));
typedef u32 u32x2 __attribute__((ext_vector_type(2)));
typedef float f32x4 __attribute__((ext_vector_type(4)));
typedef float f32x2 __attribute__((ext_vector_type(2)));
typedef __bf16 bf16x2 __attribute__((ext_vector_type(2)));
typedef __bf16 bf16x8 __attribute__((ext_vector_type(8)));

constexpr u32 T_A = 400;
constexpr int N_1 = 6;
constexpr u32 T_1 = 900;
constexpr u32 T_K = 3980;
constexpr u32 T_V = 7680;
constexpr u32 D_H = 50;
constexpr u32 T_W = 1500, D_W = 75;
constexpr u32 T_ML = 1300;
constexpr u32 T_P = 7885, D_P = 50;
constexpr u32 LAG3 = 100;
constexpr int F_X = 60;
constexpr int F_Y = 20;
constexpr int F_Z = 10;
constexpr int F_R = 6;
constexpr int F_M = 800;
constexpr u32 F_D = 256;
constexpr u32 F_0 = 1300, F_1 = F_0 + 24 * F_D;

constexpr int DM = 512, DH = 128, SK = 32768, WIN = 4096;
constexpr int NWG = 256, NTHR = 512;
constexpr int NB = 3;
constexpr int DQ = 16;
constexpr int HR = 8;
constexpr float SCALE = 0.08838834764831845f;
constexpr float LOG2E = 1.4426950408889634f;
constexpr int NBLK = SK / 16, BQ = NBLK / 7, BR = NBLK % 7;
constexpr int RMAX = (BQ + 1 + 3) / 4;

constexpr u32 WS_EPOCH = 0, WS_ML = 4096, WS_FB = 65536, WS_Y = 1048576, WS_END = 2097152;
constexpr u32 OOB = 0x80000000u;
constexpr u32 SPIN_CAP = 1u << 20;

constexpr int L_QP = 0, L_AF = L_QP + 2048, AF_ROW = 272, L_ML = L_AF + 16 * AF_ROW, L_HM = L_ML + 64, L_RED = L_HM + 64, L_CNT = L_RED + 512,
              L_LAG = L_CNT + 32, L_SC = L_LAG + 1024, SC_W = (RMAX + NB) * 64, L_END = L_SC + 4 * SC_W;

#define SB() __builtin_amdgcn_sched_barrier(0)
#define MEMBAR() asm volatile("" ::: "memory")
#define WAIT_LDS() asm volatile("s_waitcnt lgkmcnt(0)" ::: "memory")
#define WAIT_VM() asm volatile("s_waitcnt vmcnt(0)" ::: "memory")

__device__ __forceinline__ float dot2(u32 a, u32 b, float c) {
  return __builtin_amdgcn_fdot2_f32_bf16(__builtin_bit_cast(bf16x2, a), __builtin_bit_cast(bf16x2, b), c, false);
}
template <int CTRL> __device__ __forceinline__ float dpp(float x) {
  return __builtin_bit_cast(float, __builtin_amdgcn_mov_dpp(__builtin_bit_cast(int, x), CTRL, 0xf, 0xf, true));
}
constexpr int ROR8 = 0x128, ROR4 = 0x124, ROR2 = 0x122, ROR1 = 0x121;
__device__ __forceinline__ float blo(u32 w) { return __builtin_bit_cast(float, w << 16); }
__device__ __forceinline__ float bhi(u32 w) { return __builtin_bit_cast(float, w & 0xffff0000u); }
__device__ __forceinline__ float ex2(float x) { return __builtin_amdgcn_exp2f(x); }
__device__ __forceinline__ u32 fu(float x) { return __builtin_bit_cast(u32, x); }
__device__ __forceinline__ float uf(u32 x) { return __builtin_bit_cast(float, x); }
__device__ __forceinline__ u32 pk_bf16(float a, float b) {
  return __builtin_bit_cast(u32, __builtin_convertvector(f32x2{a, b}, bf16x2));
}
__device__ __forceinline__ float rs16(float a, float b) {
  auto s = __builtin_amdgcn_permlane16_swap(fu(a), fu(b), false, false);
  return uf(s[0]) + uf(s[1]);
}
__device__ __forceinline__ float rs32(float a, float b) {
  auto s = __builtin_amdgcn_permlane32_swap(fu(a), fu(b), false, false);
  return uf(s[0]) + uf(s[1]);
}
__device__ __forceinline__ float x16_max(float x) {
  auto s = __builtin_amdgcn_permlane16_swap(fu(x), fu(x), false, false);
  return fmaxf(uf(s[0]), uf(s[1]));
}
__device__ __forceinline__ float x32_max(float x) {
  auto s = __builtin_amdgcn_permlane32_swap(fu(x), fu(x), false, false);
  return fmaxf(uf(s[0]), uf(s[1]));
}
__device__ __forceinline__ void rscat8(const float (&v)[8], float& z0, float& z1) {
  const float w0 = rs32(v[0], v[4]), w1 = rs32(v[1], v[5]), w2 = rs32(v[2], v[6]), w3 = rs32(v[3], v[7]);
  z0 = rs16(w0, w2); z1 = rs16(w1, w3);
}

__device__ __forceinline__ f32x4 score_tile(const u32x4 (&K)[4], const u32x4 q, int nv) {
  float s[4];
#pragma unroll
  for (int u = 0; u < 4; ++u) {
    float d = dot2(K[u].x, q.x, 0.f);
    d = dot2(K[u].y, q.y, d); d = dot2(K[u].z, q.z, d); d = dot2(K[u].w, q.w, d);
    s[u] = d;
  }
#pragma unroll
  for (int u = 0; u < 4; ++u) s[u] += dpp<ROR8>(s[u]);
#pragma unroll
  for (int u = 0; u < 4; ++u) s[u] += dpp<ROR4>(s[u]);
#pragma unroll
  for (int u = 0; u < 4; ++u) s[u] += dpp<ROR2>(s[u]);
#pragma unroll
  for (int u = 0; u < 4; ++u) s[u] += dpp<ROR1>(s[u]);
#pragma unroll
  for (int u = 0; u < 4; u += 2) {
    const u32 r1 = pk_bf16(s[u], s[u + 1]);
    const u32 r2 = pk_bf16(blo(r1) * SCALE, bhi(r1) * SCALE);
    s[u] = blo(r2); s[u + 1] = bhi(r2);
  }
  return f32x4{s[0], nv > 1 ? s[1] : -INFINITY, nv > 2 ? s[2] : -INFINITY, nv > 3 ? s[3] : -INFINITY};
}
__device__ __forceinline__ u32x2 probs(const f32x4 s, float mL) {
  return u32x2{pk_bf16(ex2(fmaf(s.x, LOG2E, -mL)), ex2(fmaf(s.y, LOG2E, -mL))), pk_bf16(ex2(fmaf(s.z, LOG2E, -mL)), ex2(fmaf(s.w, LOG2E, -mL)))};
}
__device__ __forceinline__ void pv_tile(const u32x4 (&V)[4], const u32x2 p, float (&acc)[8]) {
#pragma unroll
  for (int u = 0; u < 4; u += 2) {
    const u32 pp = p[u >> 1];
    const u32 lo0 = pp & 0xffffu, hi0 = pp << 16, lo1 = pp >> 16, hi1 = pp & 0xffff0000u;
#pragma unroll
    for (int k = 0; k < 4; ++k) {
      acc[2 * k] = dot2(V[u][k], lo0, acc[2 * k]);
      acc[2 * k + 1] = dot2(V[u][k], hi0, acc[2 * k + 1]);
    }
#pragma unroll
    for (int k = 0; k < 4; ++k) {
      acc[2 * k] = dot2(V[u + 1][k], lo1, acc[2 * k]);
      acc[2 * k + 1] = dot2(V[u + 1][k], hi1, acc[2 * k + 1]);
    }
  }
}
__device__ __forceinline__ float max4(const f32x4 s, float m) { return fmaxf(fmaxf(m, fmaxf(s.x, s.y)), fmaxf(s.z, s.w)); }

__device__ __forceinline__ void lds_arrive(u32* cnt, int lane) {
  MEMBAR();
  if (lane == 0) __hip_atomic_fetch_add(cnt, 1u, __ATOMIC_RELAXED, __HIP_MEMORY_SCOPE_WORKGROUP);
  MEMBAR();
}
template <int SLEEP = 1> __device__ __forceinline__ void lds_wait(u32* cnt, u32 n) {
#pragma unroll 1
  for (u32 spin = 0; spin < (1u << 24); ++spin) {
    if (__hip_atomic_load(cnt, __ATOMIC_RELAXED, __HIP_MEMORY_SCOPE_WORKGROUP) >= n) break;
    __builtin_amdgcn_s_sleep(SLEEP);
  }
  MEMBAR();
}

__device__ __forceinline__ void wait_until(u32 tbase, u32 t) {
#pragma unroll 1
  for (int it = 0; it < 4096; ++it) {
    if ((u32)wall_clock64() - tbase >= t) break;
    __builtin_amdgcn_s_sleep(1);
  }
}

struct Tile { u32x4 r[4]; };
__device__ __forceinline__ void tile_issue(Tile& n, u32 early, u32 when, u32x4 rs, u32 so, u32 o0, u32 o1, u32 o2, u32 o3) {
  asm volatile(
      "s_cmp_lg_u32 %4, %11\n\t"
      "s_cbranch_scc1 1f\n\t"
      "buffer_load_dwordx4 %0, %7, %5, %6 offen nt\n\t"
      "buffer_load_dwordx4 %1, %8, %5, %6 offen nt\n\t"
      "buffer_load_dwordx4 %2, %9, %5, %6 offen nt\n\t"
      "buffer_load_dwordx4 %3, %10, %5, %6 offen nt\n"
      "1:"
      : "+v"(n.r[0]), "+v"(n.r[1]), "+v"(n.r[2]), "+v"(n.r[3])
      : "s"(early), "s"(rs), "s"(so), "v"(o0), "v"(o1), "v"(o2), "v"(o3), "n"(when)
      : "scc", "memory");
}
__device__ __forceinline__ void tile_wait(Tile& c, u32 early) {
  asm volatile(
      "s_cmp_lg_u32 %4, 0\n\t"
      "s_waitcnt vmcnt(8)\n\t"
      "s_cbranch_scc1 1f\n\t"
      "s_waitcnt vmcnt(4)\n"
      "1:"
      : "+v"(c.r[0]), "+v"(c.r[1]), "+v"(c.r[2]), "+v"(c.r[3])
      : "s"(early)
      : "scc", "memory");
}

__device__ __forceinline__ void poll_issue(u32x4& a, u32x4& b, u32x4 rs, u32 o0, u32 o1) {
  asm volatile(
      "buffer_load_dwordx4 %0, %2, %4, 0 offen sc1\n\t"
      "buffer_load_dwordx4 %1, %3, %4, 0 offen sc1"
      : "+v"(a), "+v"(b) : "v"(o0), "v"(o1), "s"(rs) : "memory");
}
__device__ __forceinline__ bool poll_take(u32x4& a, u32x4& b, u32 tag, u32& o0, u32& o1, float& y0, float& y1) {
  asm volatile("s_waitcnt vmcnt(0)" : "+v"(a), "+v"(b) :: "memory");
  const bool k0 = a.y == tag && a.w == tag && o0 != OOB, k1 = b.y == tag && b.w == tag && o1 != OOB;
  y0 += (k0 ? uf(a.x) : 0.f) + (k1 ? uf(b.x) : 0.f);
  y1 += (k0 ? uf(a.z) : 0.f) + (k1 ? uf(b.z) : 0.f);
  o0 = k0 ? OOB : o0;
  o1 = k1 ? OOB : o1;
  asm volatile("" : "+v"(y0), "+v"(y1));
  return __all((o0 & o1) == OOB);
}

struct Args {
  const void* p[2][8];
};

__device__ __forceinline__ void park(unsigned char* lds, int wave, int lane, const float (&acc)[8]) {
  float z0, z1;
  rscat8(acc, z0, z1);
  const u32 hi = pk_bf16(z0, z1);
  const u32 lo = pk_bf16(z0 - blo(hi), z1 - bhi(hi));
  unsigned char* p = lds + L_AF + (2 * wave) * AF_ROW + 16 * (lane & 15) + 4 * (lane >> 4);
  *reinterpret_cast<u32*>(p) = hi;
  *reinterpret_cast<u32*>(p + AF_ROW) = lo;
}
__device__ __forceinline__ u32x4 read_q(const unsigned char* lds, int c) {
  f32x4 lo = *reinterpret_cast<const f32x4*>(lds + L_QP + 32 * c), hi = *reinterpret_cast<const f32x4*>(lds + L_QP + 32 * c + 16);
#pragma unroll
  for (int w = 1; w < 4; ++w) {
    lo += *reinterpret_cast<const f32x4*>(lds + L_QP + w * 512 + 32 * c);
    hi += *reinterpret_cast<const f32x4*>(lds + L_QP + w * 512 + 32 * c + 16);
  }
  return u32x4{pk_bf16(lo.x, lo.y), pk_bf16(lo.z, lo.w), pk_bf16(hi.x, hi.y), pk_bf16(hi.z, hi.w)};
}

__global__ void __launch_bounds__(NTHR, 2) fused_decode(Args a) {
  __shared__ __attribute__((aligned(16))) unsigned char lds[L_END];
  const int tid = threadIdx.x, lane = tid & 63, wave = __builtin_amdgcn_readfirstlane(tid >> 6);
  const int c = lane & 15, r = lane >> 4;
  const int b = blockIdx.x, xg = b & 7, j = b >> 3;
  const int h = 4 * xg + (j >> 3), s = j & 7;
  const int pidx = h * 8 + s;
  const bool isB = s == 7;
  u32* cnt = reinterpret_cast<u32*>(lds + L_CNT);
  const void* const* in = a.p[isB ? 1 : 0];
  const unsigned short* X = (const unsigned short*)in[0];
  const unsigned short* KC = (const unsigned short*)in[1];
  const unsigned short* VC = (const unsigned short*)in[2];
  const unsigned short* WQ = (const unsigned short*)in[3] + 128 * h;
  const unsigned short* WO = (const unsigned short*)in[4] + (size_t)128 * h * DM;
  unsigned short* const out = (unsigned short*)in[5];
  unsigned char* const ws = (unsigned char*)in[6];
  asm volatile("" :: "s"(X), "s"(KC), "s"(VC), "s"(WQ), "s"(WO), "s"(ws), "s"(out));
  const u32 tbase = (u32)wall_clock64();
  const int nblk = isB ? WIN / 16 : BQ + (s < BR ? 1 : 0);
  const int R = (nblk + 3) >> 2;
  const size_t row0 = isB ? (size_t)(h >> 2) * SK + (SK - WIN) : (size_t)h * SK + (size_t)(s * BQ + (s < BR ? s : BR)) * 16;
  const int TK = (R - HR + NB - 1) / NB * NB, HS = R - TK;
  const unsigned short* const KB = KC + row0 * DH;
  const unsigned short* const VB = VC + row0 * DH;
  const auto rK = __builtin_amdgcn_make_buffer_rsrc((void*)KB, 0, (u32)HS * 16384u, 0x00020000);
  const auto rW = __builtin_amdgcn_make_buffer_rsrc((void*)ws, 0, WS_END, 0x00020000);
  const u32 vo0 = (u32)(wave & 3) * 1024u + lane * 16, vo1 = vo0 + 4096u, vo2 = vo0 + 8192u, vo3 = vo0 + 12288u;
  unsigned char* const sc = lds + L_SC + (wave & 3) * SC_W + 16 * r;

  if (wave < 4) {
    const auto rQ = __builtin_amdgcn_make_buffer_rsrc((void*)WQ, 0, (u32)(DM * 4096 - 128 * h) * 2u, 0x00020000);
    u32x2 xq[8];
    u32x4 wq[32];
    u32 qoff[8];
    const int rg = tid >> 4;
#pragma unroll
    for (int q = 0; q < 8; ++q) qoff[q] = (u32)(32 * rg + 4 * ((q + s) & 7)) * 8192u + 16u * c;
    auto ldq = [&](int i) { wq[i] = __builtin_amdgcn_raw_buffer_load_b128(rQ, qoff[i >> 2], (i & 3) * 8192, 2); };
    auto ldx = [&]() {
#pragma unroll
      for (int q = 0; q < 8; ++q) xq[q] = __builtin_nontemporal_load(reinterpret_cast<const u32x2*>(X + 32 * rg + 4 * ((q + s) & 7)));
    };
    Tile tb[NB];
    const u32x4 sK = {(u32)(u64)KB, (u32)((u64)KB >> 32) & 0xffffu, (u32)nblk * 4096u, 0x00020000u};
    const u32x4 sV = {(u32)(u64)VB, (u32)((u64)VB >> 32) & 0xffffu, (u32)nblk * 4096u, 0x00020000u};
#pragma unroll
    for (int i = 0; i < DQ; ++i) ldq(i);
    SB();
    ldx();
    SB();
    __builtin_amdgcn_s_barrier();
    SB();
    {
      float qa[8];
#pragma unroll
      for (int k = 0; k < 8; ++k) qa[k] = 0.f;
#pragma unroll
      for (int i0 = 0; i0 < 32; i0 += 4) {
#pragma unroll
        for (int i = i0; i < i0 + 4; ++i) {
          const u32 xw = xq[i >> 2][(i & 3) >> 1];
          const u32 lo = (i & 1) ? xw >> 16 : xw & 0xffffu, hi = (i & 1) ? xw & 0xffff0000u : xw << 16;
#pragma unroll
          for (int k = 0; k < 4; ++k) { qa[2 * k] = dot2(wq[i][k], lo, qa[2 * k]); qa[2 * k + 1] = dot2(wq[i][k], hi, qa[2 * k + 1]); }
        }
        SB();
        if (i0 + DQ < 32) {
#pragma unroll
          for (int i = i0 + DQ; i < i0 + DQ + 4; ++i) ldq(i);
        }
        if (i0 + DQ == 32) {
          const auto rk = __builtin_amdgcn_make_buffer_rsrc((void*)KB, 0, (u32)nblk * 4096u, 0x00020000);
          tb[0].r[0] = __builtin_amdgcn_raw_buffer_load_b128(rk, vo0, HS * 16384, 2); tb[0].r[1] = __builtin_amdgcn_raw_buffer_load_b128(rk, vo1, HS * 16384, 2);
          tb[0].r[2] = __builtin_amdgcn_raw_buffer_load_b128(rk, vo2, HS * 16384, 2); tb[0].r[3] = __builtin_amdgcn_raw_buffer_load_b128(rk, vo3, HS * 16384, 2);
        }
        SB();
      }
      float z0, z1;
      rscat8(qa, z0, z1);
      *reinterpret_cast<f32x2*>(lds + L_QP + wave * 512 + 32 * c + 8 * r) = f32x2{z0, z1};
    }
    lds_arrive(cnt, lane);
    lds_wait(cnt, 4);
    const u32x4 q = read_q(lds, c);
    const u32 dK = isB ? (T_K - T_A) * 256u / (WIN / 64 - HR + 1) : (T_K - T_1) * 256u / (RMAX - HR - N_1);
    const u32 d1 = isB ? dK : (T_1 - T_A) * 256u / N_1;
    const u32 dV = isB ? (T_V - T_K) * 256u / (WIN / 64) : (T_V - T_K) * 256u / RMAX;
    u32 dl = T_A * 256u + d1;
    int put = 0;
#pragma unroll
    for (int i = 0; i < 4; ++i) tb[1].r[i] = tb[2].r[i] = u32x4{0, 0, 0, 0};
    asm volatile("" : "+v"(tb[0].r[0]), "+v"(tb[0].r[1]), "+v"(tb[0].r[2]), "+v"(tb[0].r[3]));
    wait_until(tbase, dl >> 8);
    tile_issue(tb[1], 0, 0, sK, (u32)(HS + 1) * 16384u, vo0, vo1, vo2, vo3);
    u32 early = 0;
    float m = -INFINITY;
    auto step_end = [&](Tile& n, u64 clk, u32 shv, bool pace, const u32x4 rs, u32 so, int left) {
      asm volatile("" : "+s"(clk), "+v"(shv));
      const u32 now = (u32)clk - tbase;
      const int lag = (int)(now - (dl >> 8));
      put = min((int)__builtin_amdgcn_readfirstlane(shv), max(put + F_R, lag));
      const u32 due = (dl >> 8) + put;
      if (now < due && pace) wait_until(tbase, due);
      tile_issue(n, early, 0, rs, so, vo0, vo1, vo2, vo3);
      early = ((due + LAG3 - now) | (u32)left) >> 31;
      reinterpret_cast<int*>(lds + L_LAG)[tid] = lag;
      SB();
    };
    int t0 = 0;
    do {
#pragma unroll
      for (int bi = 0; bi < NB; ++bi) {
        const int t = t0 + bi;
        const u64 clk = wall_clock64();
        const u32 shv = __hip_atomic_load(cnt + 6, __ATOMIC_RELAXED, __HIP_MEMORY_SCOPE_WORKGROUP);
        const bool k = t + 2 < TK;
        dl += k ? (t + 2 <= N_1 ? d1 : dK) : dV;
        const u32x4 rs = {k ? sK.x : sV.x, k ? sK.y : sV.y, sK.z, sK.w};
        const u32 so = (u32)(k ? t + 2 + HS : t + 2 - TK) * 16384u;
        SB();
        tile_issue(tb[(bi + 2) % NB], early, 1, rs, so, vo0, vo1, vo2, vo3);
        tile_wait(tb[bi], early);
        const f32x4 sv = score_tile(tb[bi].r, q, nblk - 4 * (t + HS));
        m = max4(sv, m);
        if (c == 0) *reinterpret_cast<f32x4*>(sc + (t + HS) * 64) = sv;
        SB();
        step_end(tb[(bi + 2) % NB], clk, shv, true, rs, so, 0);
      }
      t0 += NB;
    } while (t0 < TK);
    lds_wait(cnt + 1, 4);
    m = fmaxf(m, reinterpret_cast<const float*>(lds + L_HM)[wave * 4 + r]);
    const float mL = x32_max(x16_max(m)) * LOG2E;
    if (lane == 0) reinterpret_cast<float*>(lds + L_ML)[wave] = mL;
    lds_arrive(cnt + 4, lane);
    float acc[8];
#pragma unroll
    for (int k = 0; k < 8; ++k) acc[k] = 0.f;
    t0 = 0;
    for (;;) {
#pragma unroll
      for (int bi = 0; bi < NB; ++bi) {
        const int t = t0 + bi;
        const u64 clk = wall_clock64();
        const u32 shv = __hip_atomic_load(cnt + 6, __ATOMIC_RELAXED, __HIP_MEMORY_SCOPE_WORKGROUP);
        const f32x4 sv = *reinterpret_cast<const f32x4*>(sc + t * 64);
        dl += dV;
        const u32 so = (u32)(t + 2) * 16384u;
        SB();
        tile_issue(tb[(bi + 2) % NB], early, 1, sV, so, vo0, vo1, vo2, vo3);
        SB();
        u32x2 p = probs(sv, mL);
        asm volatile("" : "+v"(p));
        tile_wait(tb[bi], early);
        pv_tile(tb[bi].r, p, acc);
        asm volatile("" : "+v"(acc[0]), "+v"(acc[1]), "+v"(acc[2]), "+v"(acc[3]), "+v"(acc[4]), "+v"(acc[5]), "+v"(acc[6]), "+v"(acc[7]));
        SB();
        if (t + 1 >= R) goto vdone;
        step_end(tb[(bi + 2) % NB], clk, shv, t + 2 < (nblk >> 2), sV, so, (nblk >> 2) - 4 - t);
      }
      t0 += NB;
    }
  vdone:
    asm volatile("s_waitcnt vmcnt(0)"
                 : "+v"(tb[0].r[0]), "+v"(tb[0].r[1]), "+v"(tb[0].r[2]), "+v"(tb[0].r[3]), "+v"(tb[1].r[0]), "+v"(tb[1].r[1]), "+v"(tb[1].r[2]),
                   "+v"(tb[1].r[3]), "+v"(tb[2].r[0]), "+v"(tb[2].r[1]), "+v"(tb[2].r[2]), "+v"(tb[2].r[3])
                 :: "memory");
    park(lds, wave, lane, acc);
    WAIT_LDS();
    __builtin_amdgcn_s_barrier();
  } else {
    const int hw = wave - 4, th = tid - 256;
    u32x2 ep;
    u32 tch = 0;
    {
      u32x4 kk[HR][4];
#pragma unroll
      for (int t = 0; t < HR; ++t) {
        if (t == 1) {
          ep = __builtin_amdgcn_raw_buffer_load_b64(rW, WS_EPOCH, 0, 16);
          if (th < 8) cnt[th] = 0;
          reinterpret_cast<int*>(lds + L_LAG)[th] = 0;
          WAIT_LDS();
          SB();
          __builtin_amdgcn_s_barrier();
          SB();
        }
        if (t >= 1) { wait_until(tbase, t * D_H); SB(); }
        kk[t][0] = __builtin_amdgcn_raw_buffer_load_b128(rK, vo0, t * 16384, 2); kk[t][1] = __builtin_amdgcn_raw_buffer_load_b128(rK, vo1, t * 16384, 2);
        kk[t][2] = __builtin_amdgcn_raw_buffer_load_b128(rK, vo2, t * 16384, 2); kk[t][3] = __builtin_amdgcn_raw_buffer_load_b128(rK, vo3, t * 16384, 2);
        SB();
        if (t == HR - 1) {
          const unsigned short* pt = hw == 0 ? VB : hw == 1 ? VB + ((size_t)nblk * 2048 - 2) : hw == 2 ? WO : out;
          if (lane == 0) tch = __builtin_nontemporal_load(reinterpret_cast<const u32*>(pt));
          SB();
        }
      }
      SB();
      lds_wait(cnt, 4);
      const u32x4 q = read_q(lds, c);
      float m = -INFINITY;
#pragma unroll
      for (int t = 0; t < HR; ++t) {
        const f32x4 sv = score_tile(kk[t], q, 4);
        if (t < HR - NB + 1 || t < HS) {
          m = max4(sv, m);
          if (c == 0) *reinterpret_cast<f32x4*>(sc + t * 64) = sv;
        }
        SB();
      }
      if (c == 0) reinterpret_cast<float*>(lds + L_HM)[hw * 4 + r] = m;
      WAIT_LDS();
      lds_arrive(cnt + 1, lane);
    }
    asm volatile("" :: "v"(tch));
    SB();
    const u64 tag64 = (((u64)ep.y << 32) | ep.x) + 1;
    const u32 tag32 = (u32)tag64 | 0x80000000u;
    if (th == 0) { cnt[2] = (u32)tag64; cnt[3] = (u32)(tag64 >> 32); }
    bool fbw = hw == 0 && (!isB || b < 64);
    int shift = 0;
    u32 seen = OOB;
    auto pump = [&]() {
      if (!fbw) return;
      const u32 clk = (u32)wall_clock64(), now = clk - tbase, per = clk / F_D;
      if (per == seen || now < F_0) return;
      if (now >= F_1) { fbw = false; return; }
      MEMBAR();
      seen = per;
      const int* lg = reinterpret_cast<const int*>(lds + L_LAG);
      const int lag = ((lg[0] + lg[64]) + (lg[128] + lg[192])) >> 2;
      __builtin_amdgcn_raw_buffer_store_b64(u32x2{tbase + (u32)lag, tag32}, rW,
                                            lane == 0 && s == 0 && lag - shift > F_X ? WS_FB + ((per & 63u) * 32u + (u32)h) * 8u : OOB, 0, 16);
      const u32x2 v = __builtin_amdgcn_raw_buffer_load_b64(rW, lane < 32 ? WS_FB + (((per - 1u) & 63u) * 32u + (u32)lane) * 8u : OOB, 0, 16);
      float ex = v.y == tag32 ? (float)max((int)(v.x - tbase) - shift - F_Y, -F_Z) : (float)-F_Z;
      ex += dpp<ROR8>(ex); ex += dpp<ROR4>(ex); ex += dpp<ROR2>(ex); ex += dpp<ROR1>(ex);
      ex = rs16(ex, ex);
      shift = min(shift + max(__builtin_amdgcn_readfirstlane((int)(ex * (1.f / 32.f))), 0), F_M);
      if (lane == 0) __hip_atomic_store(cnt + (isB ? 7 : 6), (u32)shift, __ATOMIC_RELAXED, __HIP_MEMORY_SCOPE_WORKGROUP);
    };
    u32x4 wo[4][8];
    {
      const auto rO = __builtin_amdgcn_make_buffer_rsrc((void*)WO, 0, 128u * 1024u, 0x00020000);
      const u32 vo = (u32)r * 8192u + (u32)hw * 256u + 16u * c;
#pragma unroll
      for (int st = 0; st < 4; ++st)
#pragma unroll
        for (int i = 0; i < 8; ++i) {
          SB(); wait_until(tbase, T_W + (8 * st + i) * D_W); SB();
          wo[st][i] = __builtin_amdgcn_raw_buffer_load_b128(rO, vo + (i & 3) * 1024, (st * 32 + (i & 4)) * 1024, 2);
          if ((i & 1) == 0) { SB(); pump(); SB(); }
        }
    }
    if (b < 64 && th < 8) {
      const int pc = 4 * b + (th & 3);
      const auto rY = __builtin_amdgcn_make_buffer_rsrc((void*)out, 0, 2048u, 0x00020000);
      __builtin_amdgcn_raw_buffer_store_b32(0u, rY, 2u * (512 * (th >> 2) + 128 * (pc >> 6) + 8 * (pc & 15) + 2 * ((pc >> 4) & 3)), 0, 0);
      WAIT_VM();
    }
    SB();
    u32x4 wf[4][8];
#pragma unroll
    for (int st = 0; st < 4; ++st)
#pragma unroll
      for (int t = 0; t < 8; ++t)
#pragma unroll
        for (int k = 0; k < 4; ++k)
          wf[st][t][k] = __builtin_amdgcn_perm(wo[st][2 * k + 1][t >> 1], wo[st][2 * k][t >> 1], (t & 1) ? 0x07060302u : 0x05040100u);
    SB();
    float w0, w1;
    {
#pragma unroll 1
      for (u32 spin = 0; spin < (1u << 22); ++spin) {
        if (__hip_atomic_load(cnt + 4, __ATOMIC_RELAXED, __HIP_MEMORY_SCOPE_WORKGROUP) >= 4) break;
        pump();
        __builtin_amdgcn_s_sleep(8);
      }
      MEMBAR();
      const f32x4 ma = *reinterpret_cast<const f32x4*>(lds + L_ML);
      const f32x2 mr = *reinterpret_cast<const f32x2*>(lds + L_ML + 8 * (r & 1));
      const float mL = reinterpret_cast<const float*>(lds + L_ML)[hw];
      float M = fmaxf(fmaxf(ma.x, ma.y), fmaxf(ma.z, ma.w)), L = 0.f;
      for (int t = c; t < R; t += 16) {
        const u32x2 p = probs(*reinterpret_cast<const f32x4*>(sc + t * 64), mL);
        L += (blo(p.x) + bhi(p.x)) + (blo(p.y) + bhi(p.y));
      }
      L += dpp<ROR8>(L); L += dpp<ROR4>(L); L += dpp<ROR2>(L); L += dpp<ROR1>(L);
      L = rs16(L, L);
      L = rs32(L, L);
      if (lane == 0) reinterpret_cast<float*>(lds + L_ML)[8 + hw] = L * ex2(mL - M);
      lds_arrive(cnt + 5, lane);
      lds_wait<8>(cnt + 5, 4);
      const f32x4 la = *reinterpret_cast<const f32x4*>(lds + L_ML + 32);
      L = (la.x + la.y) + (la.z + la.w);
      if (!isB) {
        __builtin_amdgcn_raw_buffer_store_b128(u32x4{fu(M), tag32, fu(L), tag32}, rW, th == 0 ? WS_ML + (u32)pidx * 16u : OOB, 0, 16);
        const u32 om = WS_ML + (u32)(h * 8 + (c & 7)) * 16u;
        const u32 tpub = (u32)wall_clock64();
#pragma unroll 1
        for (int i = 0; i < 256 && (u32)wall_clock64() - tpub < T_ML; ++i) { pump(); __builtin_amdgcn_s_sleep(8); }
        u32x4 e;
#pragma unroll 1
        for (u32 spin = 0;; ++spin) {
          e = __builtin_amdgcn_raw_buffer_load_b128(rW, om, 0, 16);
          if (__all((e.y == tag32 && e.w == tag32) || (c & 7) == 7) || spin > SPIN_CAP) break;
          MEMBAR();
          pump();
          __builtin_amdgcn_s_sleep(64);
        }
        const float me = (c & 7) == 7 ? -INFINITY : uf(e.x);
        M = me;
        M = fmaxf(M, dpp<ROR4>(M)); M = fmaxf(M, dpp<ROR2>(M)); M = fmaxf(M, dpp<ROR1>(M));
        L = (c & 7) == 7 ? 0.f : uf(e.z) * ex2(me - M);
        L += dpp<ROR4>(L); L += dpp<ROR2>(L); L += dpp<ROR1>(L);
      }
      w0 = r < 2 ? ex2(mr.x - M) / L : 0.f;
      w1 = r < 2 ? ex2(mr.y - M) / L : 0.f;
#pragma unroll 1
      for (int it = 0; it < 4096 && fbw; ++it) { pump(); __builtin_amdgcn_s_sleep(8); }
    }
    SB();
    WAIT_LDS();
    __builtin_amdgcn_s_barrier();
    {
      u32x4 af[4];
#pragma unroll
      for (int st = 0; st < 4; ++st) af[st] = *reinterpret_cast<const u32x4*>(lds + L_AF + (c & 7) * AF_ROW + st * 64 + 16 * r);
      float y[8];
      asm volatile("" : "+v"(af[0]), "+v"(af[1]), "+v"(af[2]), "+v"(af[3]));
      f32x4 d[8];
#pragma unroll
      for (int st = 0; st < 4; ++st) {
#pragma unroll
        for (int t = 0; t < 8; ++t)
          d[t] = __builtin_amdgcn_mfma_f32_16x16x32_bf16(__builtin_bit_cast(bf16x8, af[st]), __builtin_bit_cast(bf16x8, wf[st][t]),
                                                         st ? d[t] : f32x4{0.f, 0.f, 0.f, 0.f}, 0, 0, 0);
        SB();
      }
#pragma unroll
      for (int t = 0; t < 8; ++t) y[t] = w0 * (d[t].x + d[t].y) + w1 * (d[t].z + d[t].w);
      float z0, z1;
      rscat8(y, z0, z1);
      __builtin_amdgcn_raw_buffer_store_b128(u32x4{fu(z0), tag32, fu(z1), tag32}, rW, WS_Y + (u32)pidx * 4096u + (u32)th * 16u, 0, 16);
    }
  }
  if (b < 64) {
    u32x4 pa0 = {0, 0, 0, 0}, pa1 = pa0;
    const u32 tagl = cnt[2], tagh = cnt[3];
    const u32 tag32 = tagl | 0x80000000u;
    const u32x4 sW = {(u32)(u64)ws, (u32)((u64)ws >> 32) & 0xffffu, WS_END, 0x00020000u};
    u32 o0 = WS_Y + (u32)(tid >> 2) * 4096u + (u32)b * 64u + (tid & 3) * 16, o1 = o0 + 128 * 4096;
    float y0 = 0.f, y1 = 0.f;
    wait_until(tbase, max(T_P + (u32)__builtin_amdgcn_readfirstlane(cnt[isB ? 7 : 6]), wave < 4 ? (u32)wall_clock64() - tbase + D_P : 0u));
    poll_issue(pa0, pa1, sW, o0, o1);
#pragma unroll 1
    for (u32 spin = 0; spin <= SPIN_CAP; ++spin) {
      if (poll_take(pa0, pa1, tag32, o0, o1, y0, y1)) break;
      poll_issue(pa0, pa1, sW, o0, o1);
    }
    const bool slotB = ((tid >> 2) & 7) == 7;
    float ya0 = slotB ? 0.f : y0, ya1 = slotB ? 0.f : y1, yb0 = slotB ? y0 : 0.f, yb1 = slotB ? y1 : 0.f;
    float p0 = rs32(ya0, yb0), p1 = rs32(ya1, yb1);
    float y = rs16(p0, p1);
    y += dpp<ROR4>(y);
    y += dpp<ROR8>(y);
    if (c < 4) reinterpret_cast<float*>(lds + L_RED)[wave * 16 + (r >> 1) * 8 + 2 * c + (r & 1)] = y;
    WAIT_LDS();
    __builtin_amdgcn_s_barrier();
    if (tid < 8) {
      const f32x2* p = reinterpret_cast<const f32x2*>(lds + L_RED) + tid;
      f32x2 t = p[0];
#pragma unroll
      for (int w = 1; w < 8; ++w) t += p[8 * w];
      const int pc = 4 * b + (tid & 3);
      const auto rY = __builtin_amdgcn_make_buffer_rsrc((void*)out, 0, 2048u, 0x00020000);
      __builtin_amdgcn_raw_buffer_store_b32(pk_bf16(t.x, t.y), rY, 2u * (512 * (tid >> 2) + 128 * (pc >> 6) + 8 * (pc & 15) + 2 * ((pc >> 4) & 3)), 0, 0);
      if (b == 0 && tid == 0) __builtin_amdgcn_raw_buffer_store_b64(u32x2{tagl, tagh}, rW, WS_EPOCH, 0, 16);
    }
    asm volatile("s_waitcnt vmcnt(0)" : "+v"(pa0), "+v"(pa1) :: "memory");
  }
}

extern "C" void kernel_launch(void* const* d_in, const int* in_sizes, int n_in, void* d_out, int out_size, void* d_ws, size_t ws_size,
                              hipStream_t stream) {
  Args a;
  for (int br = 0; br < 2; ++br) {
    for (int i = 0; i < 5; ++i) a.p[br][i] = d_in[5 * br + i];
    a.p[br][5] = d_out; a.p[br][6] = d_ws; a.p[br][7] = nullptr;
  }
  hipLaunchKernelGGL(fused_decode, dim3(NWG), dim3(NTHR), 0, stream, a);
}
```
